```python
import torch

def _softmax(x, axis=-1):
    return torch.softmax(x, dim=axis)
def _gelu(x):
    return torch.nn.functional.gelu(x, approximate="tanh")
def _silu(x):
    return torch.nn.functional.silu(x)
def _sigmoid(x):
    return torch.sigmoid(x)
def _layernorm(x, axis=-1, eps=1e-5):
    m = x.mean(dim=axis, keepdim=True)
    v = x.var(dim=axis, keepdim=True, unbiased=False)
    return (x - m) / torch.sqrt(v + eps)
def _scatter_add(tgt, idx, src, axis=0):
    return tgt.clone().index_add_(axis, idx, src)
def _segment_sum(src, seg, n):
    out = torch.zeros((n,) + src.shape[1:], dtype=src.dtype)
    return out.index_add_(0, seg, src)
def _pad(x, pw):
    flat = tuple(v for pair in reversed(pw) for v in pair)
    return torch.nn.functional.pad(x, flat)

kernel_name = 'recurrent_scan__sliding_block_cached__mixer_token_mlp__decode'

def setup_inputs():

    _w = (lambda a: (lambda t: t.to(torch.bfloat16) if t.is_floating_point() else t)(torch.as_tensor(a)))
    def _A_setup():
        B, Sq, D = 4, 1, 576
        Hq, Hkv, Sk, Dh = 32, 16, 8192, 128
        assert Hq % Hkv == 0, (Hq, Hkv)
        return {
            "x": _w((lambda *s: torch.randn(*s, dtype=torch.bfloat16) * 0.05)(B, Sq, D)),
            "k_cache": _w((lambda *s: torch.randn(*s, dtype=torch.bfloat16) * 1.6)(B, Hkv, Sk, Dh)),
            "v_cache": _w((lambda *s: torch.randn(*s, dtype=torch.bfloat16) * 1.6)(B, Hkv, Sk, Dh)),
            "wq": _w((lambda *s: torch.randn(*s, dtype=torch.bfloat16) * 1.6)(D, Hq * Dh) * (D ** -0.5)),
            "wo": _w((lambda *s: torch.randn(*s, dtype=torch.bfloat16) * 1.6)(Hq * Dh, D) * ((Hq * Dh) ** -0.5)),
        }
    _A_in = _A_setup()

    def _B_setup():
        B, S, D, F = 4, 1, 576, 4608
        return {
            "x": _w((lambda *s: torch.randn(*s, dtype=torch.bfloat16) * 0.05)(B, S, D)),
            "w1": _w((lambda *s: torch.randn(*s, dtype=torch.bfloat16) * 0.2)(S, F) * (S ** -0.5)),
            "b1": _w((lambda *s: torch.randn(*s, dtype=torch.bfloat16) * 0.2)(F) * 0.02),
            "w2": _w((lambda *s: torch.randn(*s, dtype=torch.bfloat16) * 0.2)(F, S) * (F ** -0.5)),
            "b2": _w((lambda *s: torch.randn(*s, dtype=torch.bfloat16) * 0.2)(S) * 0.02),
        }
    _B_in = _B_setup()

    return {
        "A_x": _A_in["x"],
        "A_k_cache": _A_in["k_cache"],
        "A_v_cache": _A_in["v_cache"],
        "A_wq": _A_in["wq"],
        "A_wo": _A_in["wo"],
        "B_x": _B_in["x"],
        "B_w1": _B_in["w1"],
        "B_b1": _B_in["b1"],
        "B_w2": _B_in["w2"],
        "B_b2": _B_in["b2"],
    }

def reference(A_x, A_k_cache, A_v_cache, A_wq, A_wo, B_x, B_w1, B_b1, B_w2, B_b2):
    def _A_ref(x, k_cache, v_cache, wq, wo):
        B, Sq, D = x.shape
        Hq, Dh = 32, 128
        Hkv, Sk = k_cache.shape[1], k_cache.shape[2]
        W = min(4096, max(256, Sk // 4))
        rep = Hq // Hkv
        q = (lambda x, *d: x.permute(*d))((x @ wq).reshape(B, Sq, Hq, Dh), 0, 2, 1, 3)
        k = (lambda x, n, axis: torch.repeat_interleave(x, n, dim=axis))(k_cache[:, :, Sk - W:, :], rep, axis=1)
        v = (lambda x, n, axis: torch.repeat_interleave(x, n, dim=axis))(v_cache[:, :, Sk - W:, :], rep, axis=1)
        scores = torch.einsum("bhqd,bhkd->bhqk", q, k) * (Dh ** -0.5)
        p = _softmax(scores, axis=-1)
        o = torch.einsum("bhqk,bhkd->bhqd", p, v)
        return (lambda x, *d: x.permute(*d))(o, 0, 2, 1, 3).reshape(B, Sq, Hq * Dh) @ wo

    def _B_ref(x, w1, b1, w2, b2):
        B, S, D = x.shape
        h = _layernorm(x, axis=-1)
        u = torch.einsum("bsd,sf->bdf", h, w1) + b1
        u = _gelu(u)
        y = torch.einsum("bdf,fs->bds", u, w2) + b2
        return (lambda x, *d: x.permute(*d))(y, 0, 2, 1)

    x = A_x
    xn = _layernorm(x, axis=-1)
    s = _A_ref(xn, A_k_cache, A_v_cache, A_wq, A_wo)
    g = _silu(_B_ref(xn, B_w1, B_b1, B_w2, B_b2))
    return x + s * g
```

```cpp
#include <hip/hip_runtime.h>
#include <hip/hip_bf16.h>
#include <stdint.h>

typedef unsigned v4u __attribute__((ext_vector_type(4)));
typedef unsigned v2u __attribute__((ext_vector_type(2)));
typedef float f32x4 __attribute__((ext_vector_type(4)));
typedef float f32x2 __attribute__((ext_vector_type(2)));
typedef short bf16x8 __attribute__((ext_vector_type(8)));
#define LAS __attribute__((address_space(3)))
#define GAS __attribute__((address_space(1)))
#define RLX_AGENT __ATOMIC_RELAXED, __HIP_MEMORY_SCOPE_AGENT
#define RLX_WG __ATOMIC_RELAXED, __HIP_MEMORY_SCOPE_WORKGROUP

namespace {
constexpr int DM = 576, SK = 8192, WIN = 2048, FF = 4608;
constexpr int NTHR = 768;
constexpr int AUX_NT = 2, AUX_SC1 = 16, RSRC_W3 = 0x00020000;
constexpr int OOB = 0x7ffffff0;
#define WO_AUX AUX_NT
#define SM_AUX 0
constexpr unsigned SPIN_CAP = 1u << 17;
constexpr int Q_ISSUE_IT = 3, Q_CHECK_IT = 6;
constexpr int POLL1_SLEEP = 2, POLL2_SLEEP = 2, POLL2_DELAY = 8;
constexpr int WS_EPOCH = 0;
constexpr int WS_QP = 4096;
constexpr int OP_SRC = 2176;
constexpr int WS_OP = WS_QP + 16 * 4 * 3 * 2048;
constexpr int WS_SP = WS_OP + 64 * 4 * OP_SRC;
constexpr int WS_END = WS_SP + 4 * 64 * 4608;
constexpr size_t WS_STAMP = (size_t)64 << 20;
constexpr int L_K = 0;
constexpr int L_O = 131072;
constexpr int L_QP = L_O + 8192;
constexpr int L_Q = L_QP + 3072;
constexpr int L_ML = L_Q + 512;
constexpr int L_MIX = L_ML + 128;
constexpr int L_AF = L_MIX + 256;
constexpr int L_FL = L_AF + 128;
constexpr int L_BYTES = L_FL + 64;

struct Args {
  const unsigned char* x; const unsigned char* k; const unsigned char* v; const unsigned char* wq; const unsigned char* wo;
  const unsigned char* w1; const unsigned char* b1; const unsigned char* w2; const unsigned char* b2;
  unsigned char* out; unsigned char* ws;
};

__device__ __forceinline__ __amdgpu_buffer_rsrc_t mkrs(const void* p, int bytes) { return __builtin_amdgcn_make_buffer_rsrc((void*)p, 0, bytes, RSRC_W3); }
template <int AUX> __device__ __forceinline__ v4u ld16(__amdgpu_buffer_rsrc_t rs, int voff, int soff = 0) { return __builtin_amdgcn_raw_buffer_load_b128(rs, voff, soff, AUX); }
template <int AUX> __device__ __forceinline__ unsigned ld4(__amdgpu_buffer_rsrc_t rs, int voff) { return __builtin_amdgcn_raw_buffer_load_b32(rs, voff, 0, AUX); }
__device__ __forceinline__ void st16_sc1(__amdgpu_buffer_rsrc_t rs, int voff, v4u v) { __builtin_amdgcn_raw_buffer_store_b128(v, rs, voff, 0, AUX_SC1); }
__device__ __forceinline__ float asf(unsigned u) { return __builtin_bit_cast(float, u); }
__device__ __forceinline__ unsigned asu(float f) { return __builtin_bit_cast(unsigned, f); }
__device__ __forceinline__ float bflo(unsigned u) { return asf(u << 16); }
__device__ __forceinline__ float bfhi(unsigned u) { return asf(u & 0xffff0000u); }
__device__ __forceinline__ unsigned cvtpk(float lo, float hi) {
  typedef __bf16 b2 __attribute__((ext_vector_type(2)));
  b2 r = {(__bf16)lo, (__bf16)hi};
  return __builtin_bit_cast(unsigned, r);
}
template <int CTRL> __device__ __forceinline__ float dpp(float x) {
  return __builtin_bit_cast(float, __builtin_amdgcn_mov_dpp(__builtin_bit_cast(int, x), CTRL, 0xf, 0xf, true));
}
template <int CTRL> __device__ __forceinline__ unsigned dppu(unsigned x) { return (unsigned)__builtin_amdgcn_mov_dpp((int)x, CTRL, 0xf, 0xf, true); }
constexpr int XOR1 = 0xB1, XOR2 = 0x4E, XOR7 = 0x141, ROR8 = 0x128, ROR4 = 0x124;
__device__ __forceinline__ float xrow_sum(float x) {
  auto s = __builtin_amdgcn_permlane16_swap(asu(x), asu(x), false, false);
  x = asf(s[0]) + asf(s[1]);
  auto t = __builtin_amdgcn_permlane32_swap(asu(x), asu(x), false, false);
  return asf(t[0]) + asf(t[1]);
}
__device__ __forceinline__ float xrow_max(float x) {
  auto s = __builtin_amdgcn_permlane16_swap(asu(x), asu(x), false, false);
  x = fmaxf(asf(s[0]), asf(s[1]));
  auto t = __builtin_amdgcn_permlane32_swap(asu(x), asu(x), false, false);
  return fmaxf(asf(t[0]), asf(t[1]));
}
__device__ __forceinline__ f32x4 mfma(v4u a, v4u b, f32x4 c) {
  return __builtin_amdgcn_mfma_f32_16x16x32_bf16(__builtin_bit_cast(bf16x8, a), __builtin_bit_cast(bf16x8, b), c, 0, 0, 0);
}
__device__ __forceinline__ v4u colfrag(const v4u* r, int j) {
  const unsigned sel = (j & 1) ? 0x07060302u : 0x05040100u;
  v4u f;
  f[0] = __builtin_amdgcn_perm(r[1][j >> 1], r[0][j >> 1], sel);
  f[1] = __builtin_amdgcn_perm(r[3][j >> 1], r[2][j >> 1], sel);
  f[2] = __builtin_amdgcn_perm(r[5][j >> 1], r[4][j >> 1], sel);
  f[3] = __builtin_amdgcn_perm(r[7][j >> 1], r[6][j >> 1], sel);
  return f;
}
#define CBAR() asm volatile("" ::: "memory")
#define LDS_DRAIN() asm volatile("s_waitcnt lgkmcnt(0)" ::: "memory")
#define SCHED() __builtin_amdgcn_sched_barrier(0)
#define STAMP(i) do { } while (0)
#define FLUSH() do { } while (0)
#define TSARG
#define TSPASS

__device__ __forceinline__ void lds_wait_ge(volatile LAS unsigned* w, unsigned want) {
  for (unsigned spins = 0; spins < (SPIN_CAP << 3); ++spins) {
    if (__builtin_amdgcn_readfirstlane(*w) >= want) break;
    __builtin_amdgcn_s_sleep(1);
  }
  CBAR();
}

__device__ __forceinline__ v4u gather_o(__amdgpu_buffer_rsrc_t rws, unsigned tag, int pair, int split, int lane TSARG) {
  const int c = lane & 15, g = lane >> 4;
  const int half = c & 1, ph = (c >> 1) & 1, s = c >> 2;
  const int base = WS_OP + (pair * 4 + s) * OP_SRC;
  const int o0 = base + (64 * split + 8 * (4 * half + g) + 4 * ph) * 8, o1 = base + 2048 + (split >> 1) * 16;
  v4u a0, a1, ml;
  bool ok = false;
  for (unsigned spins = 0; spins < SPIN_CAP; ++spins) {
    CBAR();
    a0 = ld16<AUX_SC1>(rws, o0); a1 = ld16<AUX_SC1>(rws, o0 + 16); ml = ld16<AUX_SC1>(rws, o1);
    const bool good = (a0[1] == tag) & (a0[3] == tag) & (a1[1] == tag) & (a1[3] == tag) & (ml[1] == tag) & (ml[3] == tag);
    if (__all(good)) { ok = true; break; }
    __builtin_amdgcn_s_sleep(POLL1_SLEEP);
  }
  STAMP(7);
  const float m = asf(ml[0]);
  float M = fmaxf(m, dpp<ROR4>(m)); M = fmaxf(M, dpp<ROR8>(M));
  const float w = __builtin_amdgcn_exp2f(m - M);
  float L = w * asf(ml[2]); L += dpp<ROR4>(L); L += dpp<ROR8>(L);
  const float sc = ok ? w * __builtin_amdgcn_rcpf(L) : __builtin_nanf("");
  float x0 = asf(a0[0]) * sc, x1 = asf(a0[2]) * sc, x2 = asf(a1[0]) * sc, x3 = asf(a1[2]) * sc;
  x0 += dpp<ROR4>(x0); x1 += dpp<ROR4>(x1); x2 += dpp<ROR4>(x2); x3 += dpp<ROR4>(x3);
  x0 += dpp<ROR8>(x0); x1 += dpp<ROR8>(x1); x2 += dpp<ROR8>(x2); x3 += dpp<ROR8>(x3);
  const unsigned p0 = cvtpk(x0, x1), p1 = cvtpk(x2, x3);
  const unsigned q0 = dppu<XOR2>(p0), q1 = dppu<XOR2>(p1);
  v4u af;
  af[0] = ph ? q0 : p0; af[1] = ph ? q1 : p1; af[2] = ph ? p0 : q0; af[3] = ph ? p1 : q1;
  return af;
}
__device__ __forceinline__ void out_proj(__amdgpu_buffer_rsrc_t rws, v4u af, const v4u (&wf)[8], unsigned tag, int so, int lane) {
  f32x4 d[8];
#pragma unroll
  for (int j = 0; j < 8; ++j) d[j] = mfma(af, wf[j], f32x4{0.f, 0.f, 0.f, 0.f});
  asm volatile("" : "+v"(d[0]), "+v"(d[1]), "+v"(d[2]), "+v"(d[3]), "+v"(d[4]), "+v"(d[5]), "+v"(d[6]), "+v"(d[7]));
  const bool g0 = (lane & 16) != 0, g1 = (lane & 32) != 0;
  float y[4][2];
#pragma unroll
  for (int r = 0; r < 2; ++r) { y[0][r] = g0 ? d[2][r] : d[0][r]; y[1][r] = g0 ? d[3][r] : d[1][r]; y[2][r] = g0 ? d[6][r] : d[4][r]; y[3][r] = g0 ? d[7][r] : d[5][r]; }
  asm volatile("" : "+v"(y[0][0]), "+v"(y[1][0]), "+v"(y[2][0]), "+v"(y[3][0]), "+v"(y[0][1]), "+v"(y[1][1]), "+v"(y[2][1]), "+v"(y[3][1]));
  const float a0 = g1 ? y[2][0] : y[0][0], a1 = g1 ? y[2][1] : y[0][1], b0 = g1 ? y[3][0] : y[1][0], b1 = g1 ? y[3][1] : y[1][1];
  const float u0 = a0 + dpp<ROR8>(a1), u1 = b0 + dpp<ROR8>(b1);
  st16_sc1(rws, so, v4u{asu(u0), tag, asu(u1), tag});
}

__device__ __forceinline__ void load_wo(const Args& p, v4u (&wr)[8], int kvh, int split, int grp, int lane) {
  const int c = lane & 15, g = lane >> 4;
  const __amdgpu_buffer_rsrc_t ro = mkrs(p.wo, 4096 * DM * 2);
  const int off = (kvh * 256 + 64 * split + 8 * (4 * (c >> 3) + g)) * (DM * 2) + grp * 128 + (c & 7) * 16;
#pragma unroll
  for (int jj = 0; jj < 8; ++jj) wr[jj] = ld16<WO_AUX>(ro, off, jj * (DM * 2));
}

__global__ void __launch_bounds__(NTHR, 3) fused_kernel(Args p) {
  __shared__ __attribute__((aligned(16))) unsigned char smem[L_BYTES];
  LAS unsigned char* L = (LAS unsigned char*)smem;
  volatile LAS unsigned* fl = (volatile LAS unsigned*)(L + L_FL);
  const int tid = threadIdx.x, lane = tid & 63, wave = __builtin_amdgcn_readfirstlane(tid >> 6);
  const int c = lane & 15, g = lane >> 4;
  const int blk = blockIdx.x, idx = blk >> 3;
  const int kvh = 2 * (blk & 7) + (idx >> 4), sub = idx & 15, b = sub >> 2, split = sub & 3, pair = b * 16 + kvh;
  const __amdgpu_buffer_rsrc_t rws = mkrs(p.ws, WS_END);
  constexpr float SC = 0.088388347648318440f * 1.4426950408889634f;
  STAMP(0);
  if (tid < 4) fl[tid] = 0u;

  if (wave >= 4) {
    const int a = wave - 4;
    const size_t kvoff = ((size_t)pair * SK + (SK - WIN) + split * 512 + a * 64) * 256;
    const __amdgpu_buffer_rsrc_t rk = mkrs(p.k + kvoff, 16384), rv = mkrs(p.v + kvoff, 16384);
    const unsigned ldsk = (unsigned)(uintptr_t)(L + L_K) + a * 16384;
    {
      const int r0 = g * 256;
      const int v0 = r0 + ((c ^ (4 * g)) << 4), v1 = r0 + ((c ^ (4 * g + 1)) << 4), v2 = r0 + ((c ^ (4 * g + 2)) << 4), v3 = r0 + ((c ^ (4 * g + 3)) << 4);
      SCHED();
      __builtin_amdgcn_s_barrier();
      CBAR();
#pragma unroll
      for (int q = 0; q < 4; ++q) {
        unsigned keep;
        asm volatile("s_nop 4\n\ts_mov_b32 %0, m0\n\ts_mov_b32 m0, %1\n\ts_nop 0\n\t"
                     "buffer_load_dwordx4 %2, %6, %7 offen nt lds\n\t"
                     "buffer_load_dwordx4 %3, %6, %7 offen offset:1024 nt lds\n\t"
                     "buffer_load_dwordx4 %4, %6, %7 offen offset:2048 nt lds\n\t"
                     "buffer_load_dwordx4 %5, %6, %7 offen offset:3072 nt lds\n\t"
                     "s_mov_b32 m0, %0"
                     : "=&s"(keep) : "s"(ldsk + q * 4096), "v"(v0), "v"(v1), "v"(v2), "v"(v3), "s"(rk), "s"(q * 4096) : "memory");
      }
    }
    v4u wr[8];
    load_wo(p, wr, kvh, split, a, lane);
    SCHED();
    v4u vr[16];
#pragma unroll
    for (int i = 0; i < 16; ++i) vr[i] = ld16<AUX_NT>(rv, lane * 16 + (i & 3) * 1024, (i >> 2) * 4096);
    SCHED();
    STAMP(1);
    lds_wait_ge(fl + 1, 1u);
    const unsigned tag = __builtin_amdgcn_readfirstlane(fl[3]);
    v4u qf[4];
#pragma unroll
    for (int kk = 0; kk < 4; ++kk) qf[kk] = *(const LAS v4u*)(L + L_Q + ((c >> 2) & 1) * 256 + kk * 64 + g * 16);
    STAMP(2);
    asm volatile("s_waitcnt vmcnt(24)" ::: "memory");
    SCHED();
    f32x4 S[4];
    {
      const LAS unsigned char* kb = L + L_K + a * 16384 + (c & 3) * 1024 + (c >> 2) * 256;
#pragma unroll
      for (int t = 0; t < 4; ++t) {
        S[t] = f32x4{0.f, 0.f, 0.f, 0.f};
#pragma unroll
        for (int kk = 0; kk < 4; ++kk) {
          const v4u kf = *(const LAS v4u*)(kb + t * 4096 + (((4 * kk) ^ g ^ c) << 4));
          S[t] = mfma(kf, qf[kk], S[t]);
        }
      }
    }
    float mx = -INFINITY;
#pragma unroll
    for (int t = 0; t < 4; ++t) mx = fmaxf(mx, fmaxf(fmaxf(S[t][0], S[t][1]), fmaxf(S[t][2], S[t][3])));
    mx = xrow_max(mx) * SC;
    float ls = 0.f;
    v4u pf[2];
#pragma unroll
    for (int t = 0; t < 4; ++t) {
      float e[4];
#pragma unroll
      for (int r = 0; r < 4; ++r) { e[r] = __builtin_amdgcn_exp2f(fmaf(S[t][r], SC, -mx)); ls += e[r]; }
      pf[t >> 1][2 * (t & 1)] = cvtpk(e[0], e[1]);
      pf[t >> 1][2 * (t & 1) + 1] = cvtpk(e[2], e[3]);
    }
    ls = xrow_sum(ls);
    if ((lane & 0x3b) == 0) *(LAS f32x2*)(L + L_ML + (lane >> 2) * 64 + a * 8) = f32x2{mx, ls};
    LDS_DRAIN();
    if (lane == 0) __hip_atomic_fetch_add((LAS unsigned*)(L + L_FL + 8), 1u, RLX_WG);
    SCHED();
    STAMP(3);
    v4u wf[8];
#pragma unroll
    for (int j = 0; j < 8; ++j) { wf[j] = colfrag(wr, j); asm volatile("" : "+v"(wf[j])); }
    const int so = c < 8 ? WS_SP + (b * 64 + kvh * 4 + split) * 4608 + (64 * a + 8 * c + 2 * g) * 8 : OOB;
    SCHED();
    f32x4 acc[8];
#pragma unroll
    for (int j = 0; j < 8; ++j) acc[j] = mfma(pf[0], colfrag(vr, j), f32x4{0.f, 0.f, 0.f, 0.f});
    SCHED();
    v4u vf[8];
#pragma unroll
    for (int d = 0; d < 4; ++d) {
#pragma unroll
      for (int j = 0; j < 8; ++j) vf[j][d] = __builtin_amdgcn_perm(vr[9 + 2 * d][j >> 1], vr[8 + 2 * d][j >> 1], (j & 1) ? 0x07060302u : 0x05040100u);
      SCHED();
    }
#pragma unroll
    for (int j = 0; j < 8; ++j) acc[j] = mfma(pf[1], vf[j], acc[j]);
    asm volatile("" : "+v"(acc[0]), "+v"(acc[1]), "+v"(acc[2]), "+v"(acc[3]), "+v"(acc[4]), "+v"(acc[5]), "+v"(acc[6]), "+v"(acc[7]));
    {
      const bool hi = (lane & 32) != 0;
      *(LAS f32x4*)(L + L_O + a * 1024 + (g & 1) * 512 + c * 32 + (g >> 1) * 16) =
          f32x4{hi ? acc[4][0] : acc[0][0], hi ? acc[5][0] : acc[1][0], hi ? acc[6][0] : acc[2][0], hi ? acc[7][0] : acc[3][0]};
    }
    LDS_DRAIN();
    STAMP(4);
    SCHED();
    __builtin_amdgcn_s_barrier();
    STAMP(5);
    __builtin_amdgcn_s_barrier();
    CBAR();
    SCHED();
    STAMP(6);
    const v4u af = *(const LAS v4u*)(L + L_AF + (4 * (c & 1) + g) * 16);
    out_proj(rws, af, wf, tag, so, lane);
    STAMP(8);
    FLUSH();
    return;
  }

  __builtin_amdgcn_s_setprio(3);
  const bool qrole = (sub < 12) && (wave < 3);
  const bool cons = blk < 144;
  const int bc = blk / 36, cg = blk % 36;
  const unsigned epoch = __hip_atomic_load((const GAS unsigned*)(p.ws + WS_EPOCH), RLX_AGENT);
  const __amdgpu_buffer_rsrc_t rx = mkrs(p.x, 4 * DM * 2);
  const int xb = (lane >> 1) & 3, xc = (lane & 1) | ((lane >> 3) << 1);
  v4u xr[5];
#pragma unroll
  for (int i = 0; i < 5; ++i) xr[i] = ld16<SM_AUX>(rx, (i < 4 || xc < 8) ? xb * (DM * 2) + (xc + 16 * i) * 16 : OOB);
  const int R0 = 192 * (sub >> 2) + 64 * wave;
  const int nf = cons ? FF * 2 : 0;
  const __amdgpu_buffer_rsrc_t r1 = mkrs(p.w1, nf), rb = mkrs(p.b1, nf), r2 = mkrs(p.w2, nf);
  const unsigned xh = ld4<SM_AUX>(rx, cons ? (bc * DM + 16 * cg + 2 * (lane & 7)) * 2 : OOB);
  const unsigned m1t = ld4<SM_AUX>(r1, 8192 + tid * 4), mbt = ld4<SM_AUX>(rb, 8192 + tid * 4), m2t = ld4<SM_AUX>(r2, 8192 + tid * 4);
  const unsigned b2v = __builtin_amdgcn_raw_buffer_load_b16(mkrs(p.b2, 2), 0, 0, 0);
  v4u wr[8];
  const v4u xa = ld16<SM_AUX>(rx, qrole ? xb * (DM * 2) + (R0 + 8 * (4 * (lane & 1) + g)) * 2 : OOB);
  {
    const __amdgpu_buffer_rsrc_t rq = mkrs(p.wq, qrole ? DM * 4096 * 2 : 0);
    const int off = (R0 + 8 * (4 * (c >> 3) + g)) * 8192 + (kvh * 256 + 64 * (sub & 3)) * 2 + (c & 7) * 16;
#pragma unroll
    for (int jj = 0; jj < 8; ++jj) wr[jj] = ld16<AUX_NT>(rq, off, jj * 8192);
  }
  SCHED();
  LDS_DRAIN();
  __builtin_amdgcn_s_barrier();
  CBAR();
  v4u m1[2], mb[2], m2[2];
#pragma unroll
  for (int i = 0; i < 2; ++i) { m1[i] = ld16<SM_AUX>(r1, tid * 16, i * 4096); mb[i] = ld16<SM_AUX>(rb, tid * 16, i * 4096); m2[i] = ld16<SM_AUX>(r2, tid * 16, i * 4096); }
  SCHED();
  const unsigned tag = __builtin_amdgcn_readfirstlane(epoch) * 0x9E3779B1u + 0x7F4A7C15u;
  float mean, rstd, hs;
  {
    f32x2 xv[20];
#pragma unroll
    for (int i = 0; i < 20; ++i) xv[i] = f32x2{bflo(xr[i >> 2][i & 3]), bfhi(xr[i >> 2][i & 3])};
    f32x2 s2[4];
#pragma unroll
    for (int d = 0; d < 4; ++d) s2[d] = ((xv[d] + xv[4 + d]) + (xv[8 + d] + xv[12 + d])) + xv[16 + d];
    const f32x2 st = (s2[0] + s2[1]) + (s2[2] + s2[3]);
    float s = st[0] + st[1];
    s += dpp<XOR1>(s); s += dpp<ROR8>(s); s = xrow_sum(s);
    mean = s * (1.f / DM);
    const float mt = (xc < 8) ? mean : 0.f;
#pragma unroll
    for (int d = 0; d < 4; ++d) {
      const f32x2 e4 = xv[16 + d] - mt;
      s2[d] = e4 * e4;
#pragma unroll
      for (int i = 0; i < 4; ++i) { const f32x2 e = xv[4 * i + d] - mean; s2[d] = e * e + s2[d]; }
    }
    const f32x2 qt = (s2[0] + s2[1]) + (s2[2] + s2[3]);
    float q = qt[0] + qt[1];
    q += dpp<XOR1>(q); q += dpp<ROR8>(q); q = xrow_sum(q);
    const float var = q * (1.f / DM);
    rstd = __builtin_amdgcn_rsqf(var + 1e-5f);
    hs = rstd * __builtin_amdgcn_rsqf(var * rstd * rstd + 1e-5f);
  }
  STAMP(1);
  if (qrole) {
    v4u af;
#pragma unroll
    for (int d = 0; d < 4; ++d) af[d] = cvtpk((bflo(xa[d]) - mean) * rstd, (bfhi(xa[d]) - mean) * rstd);
    float t[8];
#pragma unroll
    for (int j = 0; j < 8; ++j) {
      const f32x4 d = mfma(af, colfrag(wr, j), f32x4{0.f, 0.f, 0.f, 0.f});
      const float e0 = d[0] + dpp<ROR8>(d[1]), e1 = d[2] + dpp<ROR8>(d[3]);
      t[j] = (g & 2) ? e1 : e0;
    }
    if (c < 8) {
      LAS f32x4* o = (LAS f32x4*)(L + L_QP + wave * 1024 + (2 * (g & 1) + (g >> 1)) * 256 + c * 32);
      o[0] = f32x4{t[0], t[1], t[2], t[3]};
      o[1] = f32x4{t[4], t[5], t[6], t[7]};
    }
    LDS_DRAIN();
    unsigned old = 0u;
    if (lane == 0) old = __hip_atomic_fetch_add((LAS unsigned*)(L + L_FL), 1u, RLX_WG);
    old = __builtin_amdgcn_readfirstlane(old);
    CBAR();
    if (old == 2u) {
#pragma unroll
      for (int i = 0; i < 2; ++i) {
        const LAS f32x2* s = (const LAS f32x2*)(L + L_QP + i * 512 + lane * 8);
        const f32x2 v = s[0] + s[128] + s[256];
        const int bb = 2 * i + (lane >> 5);
        st16_sc1(rws, WS_QP + ((kvh * 4 + bb) * 3 + (sub >> 2)) * 2048 + (64 * (sub & 3) + 2 * (lane & 31)) * 8, v4u{asu(v[0]), tag, asu(v[1]), tag});
      }
    }
  }
  STAMP(2);
  v4u qg[6];
  const int qoff = WS_QP + (kvh * 4 + b) * 3 * 2048 + lane * 16;
  __builtin_amdgcn_s_setprio(0);
  {
    const float mc = __builtin_bit_cast(float, __builtin_amdgcn_readlane(asu(mean), 2 * (bc & 3)));
    const float hc = __builtin_bit_cast(float, __builtin_amdgcn_readlane(asu(hs), 2 * (bc & 3)));
    const float h0 = (bflo(xh) - mc) * hc, h1 = (bfhi(xh) - mc) * hc;
    f32x2 w1[9], bb[9], w2[9];
#pragma unroll
    for (int i = 0; i < 8; ++i) {
      w1[i] = f32x2{bflo(m1[i >> 2][i & 3]), bfhi(m1[i >> 2][i & 3])};
      bb[i] = f32x2{bflo(mb[i >> 2][i & 3]), bfhi(mb[i >> 2][i & 3])};
      w2[i] = f32x2{bflo(m2[i >> 2][i & 3]), bfhi(m2[i >> 2][i & 3])};
    }
    w1[8] = f32x2{bflo(m1t), bfhi(m1t)}; bb[8] = f32x2{bflo(mbt), bfhi(mbt)}; w2[8] = f32x2{bflo(m2t), bfhi(m2t)};
    constexpr float C1 = -2.0f * 0.7978845608028654f * 1.4426950408889634f, C2 = C1 * 0.044715f;
    float ykeep = 0.f;
#pragma unroll 1
    for (int it = 0; it < 8; ++it) {
      if (wave == 3 && it == Q_ISSUE_IT) {
        __builtin_amdgcn_s_setprio(3);
        STAMP(8);
#pragma unroll
        for (int i = 0; i < 6; ++i) qg[i] = ld16<AUX_SC1>(rws, qoff, i * 1024);
        __builtin_amdgcn_s_setprio(0);
      }
      if (wave == 3 && it == Q_CHECK_IT) {
        __builtin_amdgcn_s_setprio(3);
        for (unsigned spins = 0; spins < SPIN_CAP; ++spins) {
          bool good = true;
#pragma unroll
          for (int i = 0; i < 6; ++i) good &= (qg[i][1] == tag) & (qg[i][3] == tag);
          if (__all(good)) break;
          __builtin_amdgcn_s_sleep(2);
          CBAR();
#pragma unroll
          for (int i = 0; i < 6; ++i) qg[i] = ld16<AUX_SC1>(rws, qoff, i * 1024);
        }
#pragma unroll
        for (int i = 0; i < 2; ++i)
          *(LAS unsigned*)(L + L_Q + i * 256 + lane * 4) = cvtpk(asf(qg[i][0]) + asf(qg[2 + i][0]) + asf(qg[4 + i][0]), asf(qg[i][2]) + asf(qg[2 + i][2]) + asf(qg[4 + i][2]));
        if (lane == 0) fl[3] = tag;
        LDS_DRAIN();
        if (lane == 0) fl[1] = 1u;
        STAMP(9);
        __builtin_amdgcn_s_setprio(0);
      }
      if (!cons) { if (it < Q_ISSUE_IT) __builtin_amdgcn_s_sleep(18); continue; }
      const float ha = __builtin_bit_cast(float, __builtin_amdgcn_readlane(asu(h0), it)), hb = __builtin_bit_cast(float, __builtin_amdgcn_readlane(asu(h1), it));
      f32x2 acca = {0.f, 0.f}, accb = {0.f, 0.f};
#pragma unroll
      for (int i = 0; i < 9; ++i) {
        const f32x2 ua = w1[i] * ha + bb[i], ub = w1[i] * hb + bb[i];
        const f32x2 za = ua * (ua * ua * C2 + C1), zb = ub * (ub * ub * C2 + C1);
        const f32x2 da = f32x2{__builtin_amdgcn_exp2f(za[0]), __builtin_amdgcn_exp2f(za[1])} + 1.0f, db = f32x2{__builtin_amdgcn_exp2f(zb[0]), __builtin_amdgcn_exp2f(zb[1])} + 1.0f;
        const f32x2 ra = {__builtin_amdgcn_rcpf(da[0]), __builtin_amdgcn_rcpf(da[1])}, rb_ = {__builtin_amdgcn_rcpf(db[0]), __builtin_amdgcn_rcpf(db[1])};
        acca = (ua * w2[i]) * ra + acca; accb = (ub * w2[i]) * rb_ + accb;
      }
      const float ya = acca[0] + acca[1], yb = accb[0] + accb[1];
      float ta = ya + dpp<XOR1>(ya), tb = yb + dpp<XOR1>(yb);
      asm volatile("" : "+v"(ta), "+v"(tb));
      float y = (lane & 1) ? tb : ta;
      y += dpp<XOR2>(y); y += dpp<ROR4>(y); y += dpp<ROR8>(y);
      ykeep = ((c >> 1) == it) ? y : ykeep;
    }
    ykeep = xrow_sum(ykeep);
    if (g == 0) *(LAS float*)(L + L_MIX + wave * 64 + c * 4) = ykeep;
  }
  STAMP(3);
  __builtin_amdgcn_s_setprio(3);
  if (wave == 3) {
    STAMP(4);
    LDS_DRAIN();
    __builtin_amdgcn_s_barrier();
    CBAR();
    if (!cons) { FLUSH(); return; }
    const LAS f32x2* mx = (const LAS f32x2*)(L + L_MIX + (lane & 7) * 8);
    const f32x2 yy = (mx[0] + mx[8]) + (mx[16] + mx[24]) + bflo(b2v);
    float g0 = yy[0] * __builtin_amdgcn_rcpf(1.f + __builtin_amdgcn_exp2f(-1.4426950408889634f * yy[0]));
    float g1 = yy[1] * __builtin_amdgcn_rcpf(1.f + __builtin_amdgcn_exp2f(-1.4426950408889634f * yy[1]));
    asm volatile("" : "+v"(g0), "+v"(g1));
    const int so = WS_SP + (bc * 64 + (lane >> 3)) * 4608 + cg * 128 + (lane & 7) * 16;
    v4u sp[8];
    bool ok = false;
    __builtin_amdgcn_s_barrier();
    __builtin_amdgcn_s_sleep(POLL2_DELAY);
    STAMP(5);
    for (unsigned spins = 0; spins < SPIN_CAP; ++spins) {
      CBAR();
#pragma unroll
      for (int i = 0; i < 8; ++i) sp[i] = ld16<AUX_SC1>(rws, so, i * 8 * 4608);
      bool good = true;
#pragma unroll
      for (int i = 0; i < 8; ++i) good &= (sp[i][1] == tag) & (sp[i][3] == tag);
      if (__all(good)) { ok = true; break; }
      __builtin_amdgcn_s_sleep(POLL2_SLEEP);
    }
    STAMP(6);
    float s0 = 0.f, s1 = 0.f;
#pragma unroll
    for (int i = 0; i < 8; ++i) { s0 += asf(sp[i][0]); s1 += asf(sp[i][2]); }
    s0 += dpp<ROR8>(s0); s1 += dpp<ROR8>(s1);
    s0 = xrow_sum(s0); s1 = xrow_sum(s1);
    if (!ok) s0 = __builtin_nanf("");
    if (lane < 8) *(unsigned*)(p.out + (bc * DM + 16 * cg + 2 * lane) * 2) = cvtpk(fmaf(s0, g0, bflo(xh)), fmaf(s1, g1, bfhi(xh)));
    if (blk == 0 && lane == 0) __hip_atomic_store((GAS unsigned*)(p.ws + WS_EPOCH), tag, RLX_AGENT);
    STAMP(7);
    FLUSH();
    return;
  }
  if (wave == 2) {
    load_wo(p, wr, kvh, split, 8, lane);
    const int so = c < 8 ? WS_SP + (b * 64 + kvh * 4 + split) * 4608 + (64 * 8 + 8 * c + 2 * g) * 8 : OOB;
    v4u wf[8];
#pragma unroll
    for (int j = 0; j < 8; ++j) { wf[j] = colfrag(wr, j); asm volatile("" : "+v"(wf[j])); }
    LDS_DRAIN();
    STAMP(4);
    __builtin_amdgcn_s_barrier();
    CBAR();
    STAMP(5);
    const v4u af = gather_o(rws, tag, pair, split, lane TSPASS);
    if (c < 2) *(LAS v4u*)(L + L_AF + (4 * c + g) * 16) = af;
    LDS_DRAIN();
    __builtin_amdgcn_s_barrier();
    out_proj(rws, af, wf, tag, so, lane);
    STAMP(8);
    FLUSH();
    return;
  }
  {
    lds_wait_ge(fl + 2, 8u);
    const f32x2 ml = *(const LAS f32x2*)(L + L_ML + wave * 64 + (lane & 7) * 8);
    float M = fmaxf(ml[0], dpp<XOR1>(ml[0])); M = fmaxf(M, dpp<XOR2>(M)); M = fmaxf(M, dpp<XOR7>(M));
    const float w = __builtin_amdgcn_exp2f(ml[0] - M);
    float Ls = w * ml[1]; Ls += dpp<XOR1>(Ls); Ls += dpp<XOR2>(Ls); Ls += dpp<XOR7>(Ls);
    const int ob = WS_OP + (pair * 4 + split) * OP_SRC;
    st16_sc1(rws, lane == 0 ? ob + 2048 + wave * 16 : OOB, v4u{asu(M), tag, asu(Ls), tag});
    float ww[8];
#pragma unroll
    for (int i = 0; i < 8; ++i) ww[i] = __builtin_bit_cast(float, __builtin_amdgcn_readlane(asu(w), i));
    LDS_DRAIN();
    STAMP(4);
    __builtin_amdgcn_s_barrier();
    CBAR();
    STAMP(5);
    const LAS f32x2* o = (const LAS f32x2*)(L + L_O + wave * 512 + lane * 8);
    f32x2 s = o[0] * ww[0];
#pragma unroll
    for (int i = 1; i < 8; ++i) s += o[i * 128] * ww[i];
    st16_sc1(rws, ob + (wave * 128 + 2 * lane) * 8, v4u{asu(s[0]), tag, asu(s[1]), tag});
    STAMP(6);
    FLUSH();
  }
}
}

extern "C" void kernel_launch(void* const* d_in, const int* in_sizes, int n_in, void* d_out, int out_size, void* d_ws, size_t ws_size,
                              hipStream_t stream) {
  Args a;
  a.x = (const unsigned char*)d_in[0]; a.k = (const unsigned char*)d_in[1]; a.v = (const unsigned char*)d_in[2];
  a.wq = (const unsigned char*)d_in[3]; a.wo = (const unsigned char*)d_in[4];
  a.w1 = (const unsigned char*)d_in[6]; a.b1 = (const unsigned char*)d_in[7]; a.w2 = (const unsigned char*)d_in[8]; a.b2 = (const unsigned char*)d_in[9];
  a.out = (unsigned char*)d_out; a.ws = (unsigned char*)d_ws;
  hipLaunchKernelGGL(fused_kernel, dim3(256), dim3(NTHR), 0, stream, a);
}
```

```cpp
#include <hip/hip_runtime.h>
#include <hip/hip_bf16.h>
#include <stdint.h>

typedef unsigned v4u __attribute__((ext_vector_type(4)));
typedef unsigned v2u __attribute__((ext_vector_type(2)));
typedef float f32x4 __attribute__((ext_vector_type(4)));
typedef float f32x2 __attribute__((ext_vector_type(2)));
typedef short bf16x8 __attribute__((ext_vector_type(8)));
#define LAS __attribute__((address_space(3)))
#define GAS __attribute__((address_space(1)))
#define RLX_AGENT __ATOMIC_RELAXED, __HIP_MEMORY_SCOPE_AGENT
#define RLX_WG __ATOMIC_RELAXED, __HIP_MEMORY_SCOPE_WORKGROUP

namespace {
constexpr int DM = 576, SK = 8192, WIN = 2048, FF = 4608;
constexpr int NTHR = 768;
constexpr int AUX_NT = 2, AUX_SC1 = 16, RSRC_W3 = 0x00020000;
constexpr int OOB = 0x7ffffff0;
#define V_AUX AUX_NT
#define K_POL "nt"
#define WO_AUX AUX_NT
#define SM_AUX 0
constexpr unsigned SPIN_CAP = 1u << 17;
#define Q_ISSUE_AT 15
#define Q_CHECK_AT 5
#define ATT_PRIO 2
#define POLL1_SLEEP 0
#define POLL2_SLEEP 0
#define PACE 4
constexpr int WS_EPOCH = 0;
constexpr int WS_QP = 4096;
#define REPL 0
constexpr int OP_CPY = 1152;
constexpr int OP_SRC = (REPL ? 4 : 1) * OP_CPY;
constexpr int WS_OP = WS_QP + 16 * 4 * 3 * 1024;
constexpr int WS_SP = WS_OP + 64 * 4 * OP_SRC;
constexpr int WS_YP = WS_SP + 4 * 64 * 2304;
constexpr int WS_END = WS_YP + 4 * 2304 * 4;
constexpr size_t WS_STAMP = (size_t)64 << 20;
constexpr int L_K = 0;
constexpr int L_O = 131072;
constexpr int L_QP = L_O + 8192;
constexpr int L_Q = L_QP + 3072;
constexpr int L_ML = L_Q + 512;
constexpr int L_MIX = L_ML + 128;
constexpr int L_X = L_MIX + 512;
constexpr int L_XN = L_X + 4608;
constexpr int L_ST = L_XN + 4608;
constexpr int L_AF = L_ST + 32;
constexpr int L_FL = L_AF + 512;
constexpr int L_BYTES = L_FL + 64;

struct Args {
  const unsigned char* x; const unsigned char* k; const unsigned char* v; const unsigned char* wq; const unsigned char* wo;
  const unsigned char* w1; const unsigned char* b1; const unsigned char* w2; const unsigned char* b2;
  unsigned char* out; unsigned char* ws;
};

__device__ __forceinline__ __amdgpu_buffer_rsrc_t mkrs(const void* p, int bytes) { return __builtin_amdgcn_make_buffer_rsrc((void*)p, 0, bytes, RSRC_W3); }
template <int AUX> __device__ __forceinline__ v4u ld16(__amdgpu_buffer_rsrc_t rs, int voff, int soff = 0) { return __builtin_amdgcn_raw_buffer_load_b128(rs, voff, soff, AUX); }
template <int AUX> __device__ __forceinline__ unsigned ld4(__amdgpu_buffer_rsrc_t rs, int voff) { return __builtin_amdgcn_raw_buffer_load_b32(rs, voff, 0, AUX); }
__device__ __forceinline__ void st16_sc1(__amdgpu_buffer_rsrc_t rs, int voff, v4u v) { __builtin_amdgcn_raw_buffer_store_b128(v, rs, voff, 0, AUX_SC1); }
__device__ __forceinline__ float asf(unsigned u) { return __builtin_bit_cast(float, u); }
__device__ __forceinline__ unsigned asu(float f) { return __builtin_bit_cast(unsigned, f); }
__device__ __forceinline__ float bflo(unsigned u) { return asf(u << 16); }
__device__ __forceinline__ float bfhi(unsigned u) { return asf(u & 0xffff0000u); }
__device__ __forceinline__ unsigned cvtpk(float lo, float hi) {
  typedef __bf16 b2 __attribute__((ext_vector_type(2)));
  b2 r = {(__bf16)lo, (__bf16)hi};
  return __builtin_bit_cast(unsigned, r);
}
template <int CTRL> __device__ __forceinline__ float dpp(float x) {
  return __builtin_bit_cast(float, __builtin_amdgcn_mov_dpp(__builtin_bit_cast(int, x), CTRL, 0xf, 0xf, true));
}
template <int CTRL> __device__ __forceinline__ unsigned dppu(unsigned x) { return (unsigned)__builtin_amdgcn_mov_dpp((int)x, CTRL, 0xf, 0xf, true); }
constexpr int XOR1 = 0xB1, XOR2 = 0x4E, XOR7 = 0x141, ROR8 = 0x128, ROR4 = 0x124;
__device__ __forceinline__ float xrow_sum(float x) {
  auto s = __builtin_amdgcn_permlane16_swap(asu(x), asu(x), false, false);
  x = asf(s[0]) + asf(s[1]);
  auto t = __builtin_amdgcn_permlane32_swap(asu(x), asu(x), false, false);
  return asf(t[0]) + asf(t[1]);
}
__device__ __forceinline__ float xrow_max(float x) {
  auto s = __builtin_amdgcn_permlane16_swap(asu(x), asu(x), false, false);
  x = fmaxf(asf(s[0]), asf(s[1]));
  auto t = __builtin_amdgcn_permlane32_swap(asu(x), asu(x), false, false);
  return fmaxf(asf(t[0]), asf(t[1]));
}
__device__ __forceinline__ f32x4 mfma(v4u a, v4u b, f32x4 c) {
  return __builtin_amdgcn_mfma_f32_16x16x32_bf16(__builtin_bit_cast(bf16x8, a), __builtin_bit_cast(bf16x8, b), c, 0, 0, 0);
}
__device__ __forceinline__ v4u colfrag(const v4u* r, int j) {
  const unsigned sel = (j & 1) ? 0x07060302u : 0x05040100u;
  v4u f;
  f[0] = __builtin_amdgcn_perm(r[1][j >> 1], r[0][j >> 1], sel);
  f[1] = __builtin_amdgcn_perm(r[3][j >> 1], r[2][j >> 1], sel);
  f[2] = __builtin_amdgcn_perm(r[5][j >> 1], r[4][j >> 1], sel);
  f[3] = __builtin_amdgcn_perm(r[7][j >> 1], r[6][j >> 1], sel);
  return f;
}
#define CBAR() asm volatile("" ::: "memory")
#define LDS_DRAIN() asm volatile("s_waitcnt lgkmcnt(0)" ::: "memory")
#define SCHED() __builtin_amdgcn_sched_barrier(0)
#define VM_WAIT(n) do { SCHED(); asm volatile("s_waitcnt vmcnt(%0)" :: "n"(n) : "memory"); SCHED(); } while (0)
#define STR2(x) #x
#define STR(x) STR2(x)
#define AF_SLEEP 0
#define POLL1_STAG 6
#define NPOLL1 3
#define NPOLL2 4
#define CODE_PF 14336
#define MIXW_AT 1
#define ATT_FIRST(x) (x)
#define V_PACED 8
#define V_PRE 12
#define PD1 2
#define PD2 1
#define POLL1_GAP 8
#define POLL2_GAP 8
#define GATE1 8
#define GATE2A 0
#define GATE2B 6
#define G2HI 128
#define G2LO 32
#define YP_SLEEP 32
#define CQROT 0
#define WQ_AUX AUX_NT
#define K_EARLY 0
#define PACEW 12
#define PACEK 4
#define PACEK_FROM 4
#define QW 3
#define QL_AUX AUX_SC1
#define PACEV PACE
#define PACEV_WAIT() do { SCHED(); asm volatile("s_waitcnt vmcnt(" STR(PACEV) ")" ::: "memory"); SCHED(); } while (0)
#define PACE_WAIT() do { SCHED(); asm volatile("s_waitcnt vmcnt(" STR(PACE) ")" ::: "memory"); SCHED(); } while (0)
#define STAMP(i) do { } while (0)
#define FLUSH() do { } while (0)

template <int SLEEP> __device__ __forceinline__ void lds_wait_ge(volatile LAS unsigned* w, unsigned want) {
#pragma unroll 1
  for (unsigned spins = 0; spins < (SPIN_CAP << 4); ++spins) {
    if (__builtin_amdgcn_readfirstlane(*w) >= want) break;
    if (SLEEP) __builtin_amdgcn_s_sleep(SLEEP);
  }
  CBAR();
}
__device__ __forceinline__ void lds_arrive(LAS unsigned char* L, int word, int lane) {
  LDS_DRAIN();
  if (lane == 0) __hip_atomic_fetch_add((LAS unsigned*)(L + L_FL + 4 * word), 1u, RLX_WG);
}
__device__ __forceinline__ v4u pack4(f32x4 x, unsigned tag) {
  return v4u{__builtin_amdgcn_perm(asu(x[0]), tag, 0x07060500u), __builtin_amdgcn_perm(asu(x[1]), tag, 0x07060501u),
             __builtin_amdgcn_perm(asu(x[2]), tag, 0x07060502u), __builtin_amdgcn_perm(asu(x[3]), tag, 0x07060503u)};
}
__device__ __forceinline__ float q24(float x) { return asf(asu(x) & 0xffffff00u); }
__device__ __forceinline__ f32x4 unpack4(v4u v) { return f32x4{asf(v[0] & 0xffffff00u), asf(v[1] & 0xffffff00u), asf(v[2] & 0xffffff00u), asf(v[3] & 0xffffff00u)}; }
__device__ __forceinline__ bool tagged(v4u v, unsigned tag) {
  const unsigned lo = __builtin_amdgcn_perm(v[1], v[0], 0x0c0c0400u), hi = __builtin_amdgcn_perm(v[3], v[2], 0x04000c0cu);
  return (lo | hi) == tag;
}

__device__ __forceinline__ void poll_dma(__amdgpu_buffer_rsrc_t rws, int voff, unsigned lds) {
  unsigned keep;
  asm volatile("s_mov_b32 %0, m0\n\ts_mov_b32 m0, %1\n\ts_nop 0\n\tbuffer_load_dwordx4 %2, %3, 0 offen sc1 lds\n\ts_mov_b32 m0, %0"
               : "=&s"(keep) : "s"(lds), "v"(voff), "s"(rws) : "memory");
}

__device__ __forceinline__ void out_proj(__amdgpu_buffer_rsrc_t rws, v4u af, const v4u (&wf)[8], unsigned tag, int so, int lane) {
  f32x4 d[8];
#pragma unroll
  for (int j = 0; j < 8; ++j) d[j] = mfma(af, wf[j], f32x4{0.f, 0.f, 0.f, 0.f});
  asm volatile("" : "+v"(d[0]), "+v"(d[1]), "+v"(d[2]), "+v"(d[3]), "+v"(d[4]), "+v"(d[5]), "+v"(d[6]), "+v"(d[7]));
  const bool up = (lane & 8) != 0;
  f32x4 u;
#pragma unroll
  for (int k = 0; k < 4; ++k) { const float keep = up ? d[4 + k][1] : d[k][0], send = up ? d[k][1] : d[4 + k][0]; u[k] = keep + dpp<ROR8>(send); }
  st16_sc1(rws, so, pack4(u, tag));
}

__global__ void __launch_bounds__(NTHR, 3) fused_kernel(Args p) {
  __shared__ __attribute__((aligned(16))) unsigned char smem[L_BYTES];
  LAS unsigned char* L = (LAS unsigned char*)smem;
  volatile LAS unsigned* fl = (volatile LAS unsigned*)(L + L_FL);
  const int tid = threadIdx.x, lane = tid & 63, wave = __builtin_amdgcn_readfirstlane(tid >> 6);
  const int c = lane & 15, g = lane >> 4;
  const int blk = blockIdx.x, idx = blk >> 3;
  const int kvh = 2 * (blk & 7) + (idx >> 4), sub = idx & 15, b = sub >> 2, split = sub & 3, pair = b * 16 + kvh;
  const __amdgpu_buffer_rsrc_t rws = mkrs(p.ws, WS_END);
  constexpr float SC = 0.088388347648318440f * 1.4426950408889634f;
  STAMP(0);
  unsigned pf = ld4<0>(mkrs((const void*)(__builtin_amdgcn_s_getpc() & ~255ull), CODE_PF), wave >= 8 ? (tid - 512) * 64 : OOB);
  SCHED();
#define PF_DONE() asm volatile("" :: "v"(pf))
  if (tid < 16) fl[tid] = 0u;
  constexpr int WQ_BYTES = DM * 4096 * 2;
  constexpr int WO_BYTES = 4096 * DM * 2;

  if (ATT_FIRST(wave >= 4)) {
    const int a = wave - 4;
    const size_t kvoff = ((size_t)pair * SK + (SK - WIN) + split * 512 + a * 64) * 256;
    const __amdgpu_buffer_rsrc_t rk = mkrs(p.k + kvoff, 16384), rv = mkrs(p.v + kvoff, 16384), ro = mkrs(p.wo, WO_BYTES);
    const unsigned ldsk = (unsigned)(uintptr_t)(L + L_K) + a * 16384;
    unsigned hh = (unsigned)(blk * 8 + a) * 0x9E3779B1u; hh ^= hh >> 15; hh *= 0x85EBCA77u; hh ^= hh >> 13;
    const int hr = (int)((hh >> 8) & 15u) << 10;
    int kv[4];
#pragma unroll
    for (int r = 0; r < 4; ++r) kv[r] = g * 256 + ((c ^ (4 * g + r)) << 4);
    const int qoff = WS_QP + (kvh * 4 + b) * 3 * 1024 + lane * 16;
    SCHED();
    __builtin_amdgcn_s_setprio(ATT_PRIO);
    v4u wr[8], qg[3];
#define K_PIECE(i) do { unsigned keep; \
      asm volatile("s_mov_b32 %0, m0\n\ts_mov_b32 m0, %1\n\ts_nop 0\n\tbuffer_load_dwordx4 %2, %3, %4 offen " K_POL " lds\n\ts_mov_b32 m0, %0" \
                   : "=&s"(keep) : "s"(ldsk + (i) * 1024), "v"(kv[(i) & 3]), "s"(rk), "s"(((i) << 10) ^ hr) : "memory"); } while (0)
    STAMP(4);
#pragma unroll
    for (int i = 0; i < K_EARLY; ++i) K_PIECE(i);
    __builtin_amdgcn_s_barrier();
    CBAR();
    const bool proj = a < 2 || (a == 2 && b == 3);
    const int grp = 2 * b + a;
    if (proj) {
      const int wo_off = (kvh * 256 + 64 * split + 8 * (4 * (c >> 3) + g)) * (DM * 2) + grp * 128 + (c & 7) * 16;
#pragma unroll
      for (int jj = 0; jj < 8; ++jj) { wr[jj] = ld16<WO_AUX>(ro, wo_off, jj * (DM * 2)); VM_WAIT(PACEW); }
    } else {
#pragma unroll
      for (int jj = 0; jj < 8; ++jj) wr[jj] = v4u{0u, 0u, 0u, 0u};
    }
    SCHED();
#pragma unroll
    for (int i = K_EARLY; i < 16; ++i) { K_PIECE(i); VM_WAIT((i - K_EARLY < PACEK && PACEW > PACEK) ? PACEW : PACEK); }
    SCHED();
    PF_DONE();
    STAMP(9);
    v4u vr[16];
    unsigned tag;
    if (a == QW) {
#pragma unroll
      for (int k = 0; k < 3; ++k) qg[k] = ld16<QL_AUX>(rws, qoff, k * 1024);
#pragma unroll
      for (int i = 0; i < Q_CHECK_AT; ++i) { vr[i] = ld16<V_AUX>(rv, lane * 16, (i << 10) ^ hr); VM_WAIT(PACEV + 3); }
      lds_wait_ge<1>(fl + 7, 4u);
      tag = __builtin_amdgcn_readfirstlane(fl[3]);
      bool good = tagged(qg[0], tag) & tagged(qg[1], tag) & tagged(qg[2], tag);
      f32x4 qs = unpack4(qg[0]) + unpack4(qg[1]) + unpack4(qg[2]);
#pragma unroll 1
      for (unsigned spins = 0; spins < SPIN_CAP && !__all(good); ++spins) {
        CBAR();
        const int qo = good ? OOB : qoff;
        v4u t[3];
#pragma unroll
        for (int k = 0; k < 3; ++k) t[k] = ld16<AUX_SC1>(rws, qo, k * 1024);
        const bool hit = !good & tagged(t[0], tag) & tagged(t[1], tag) & tagged(t[2], tag);
        const f32x4 qn = unpack4(t[0]) + unpack4(t[1]) + unpack4(t[2]);
        qs = hit ? qn : qs; good |= hit;
      }
      *(LAS v2u*)(L + L_Q + lane * 8) = v2u{cvtpk(qs[0], qs[1]), cvtpk(qs[2], qs[3])};
      lds_arrive(L, 1, lane);
    } else {
#pragma unroll
      for (int i = 0; i < Q_CHECK_AT; ++i) { vr[i] = ld16<V_AUX>(rv, lane * 16, (i << 10) ^ hr); VM_WAIT(PACEV); }
    }
    STAMP(7);
#pragma unroll
    for (int i = Q_CHECK_AT; i < V_PACED; ++i) { vr[i] = ld16<V_AUX>(rv, lane * 16, (i << 10) ^ hr); VM_WAIT(PACEV); }
#pragma unroll
    for (int i = V_PACED; i < V_PRE; ++i) vr[i] = ld16<V_AUX>(rv, lane * 16, (i << 10) ^ hr);
    SCHED();
    STAMP(1);
    lds_wait_ge<1>(fl + 1, 1u);
    tag = __builtin_amdgcn_readfirstlane(fl[3]);
    v4u qf[4];
#pragma unroll
    for (int kk = 0; kk < 4; ++kk) qf[kk] = *(const LAS v4u*)(L + L_Q + ((c >> 2) & 1) * 256 + kk * 64 + g * 16);
    STAMP(2);
    asm volatile("s_waitcnt vmcnt(" STR(V_PRE) ")" ::: "memory");
    SCHED();
    f32x4 S[4];
    {
      const LAS unsigned char* kb = L + L_K + a * 16384 + (c & 3) * 1024 + (c >> 2) * 256;
#pragma unroll
      for (int t = 0; t < 4; ++t) S[t] = f32x4{0.f, 0.f, 0.f, 0.f};
#pragma unroll
      for (int kk = 0; kk < 4; ++kk) {
        v4u kf[4];
#pragma unroll
        for (int t = 0; t < 4; ++t) kf[t] = *(const LAS v4u*)(kb + t * 4096 + (((4 * kk) ^ g ^ c) << 4));
#pragma unroll
        for (int t = 0; t < 4; ++t) S[t] = mfma(kf[t], qf[kk], S[t]);
      }
    }
    SCHED();
#pragma unroll
    for (int i = V_PRE; i < 16; ++i) vr[i] = ld16<V_AUX>(rv, lane * 16, (i << 10) ^ hr);
    SCHED();
    float mx = -INFINITY;
#pragma unroll
    for (int t = 0; t < 4; ++t) mx = fmaxf(mx, fmaxf(fmaxf(S[t][0], S[t][1]), fmaxf(S[t][2], S[t][3])));
    mx = xrow_max(mx) * SC;
    float ls = 0.f;
    v4u pf[2];
#pragma unroll
    for (int t = 0; t < 4; ++t) {
      float e[4];
#pragma unroll
      for (int r = 0; r < 4; ++r) e[r] = __builtin_amdgcn_exp2f(fmaf(S[t][r], SC, -mx));
      ls += (e[0] + e[1]) + (e[2] + e[3]);
      pf[t >> 1][2 * (t & 1)] = cvtpk(e[0], e[1]);
      pf[t >> 1][2 * (t & 1) + 1] = cvtpk(e[2], e[3]);
    }
    ls = xrow_sum(ls);
    if ((lane & 0x3b) == 0) *(LAS f32x2*)(L + L_ML + (lane >> 2) * 64 + a * 8) = f32x2{mx, ls};
    lds_arrive(L, 2, lane);
    SCHED();
    STAMP(3);
    f32x4 acc[8];
#pragma unroll
    for (int j = 0; j < 8; ++j) acc[j] = mfma(pf[0], colfrag(vr, j), f32x4{0.f, 0.f, 0.f, 0.f});
    SCHED();
    v4u wf[8];
#pragma unroll
    for (int j = 0; j < 8; ++j) { wf[j] = colfrag(wr, j); asm volatile("" : "+v"(wf[j])); }
    int so = WS_SP + (kvh * 4 + split) * 9216 + grp * 1024 + g * 256 + (c & 7) * 32 + (c >> 3) * 16;
    unsigned oaddr = L_O + a * 1024 + (g & 1) * 512 + c * 32 + (g >> 1) * 16;
    asm volatile("" : "+v"(so), "+v"(oaddr));

    SCHED();
    v4u vf[8];
#pragma unroll
    for (int d = 0; d < 4; ++d) {
#pragma unroll
      for (int j = 0; j < 8; ++j) vf[j][d] = __builtin_amdgcn_perm(vr[9 + 2 * d][j >> 1], vr[8 + 2 * d][j >> 1], (j & 1) ? 0x07060302u : 0x05040100u);
      SCHED();
    }
#pragma unroll
    for (int j = 0; j < 8; ++j) acc[j] = mfma(pf[1], vf[j], acc[j]);
    asm volatile("" : "+v"(acc[0]), "+v"(acc[1]), "+v"(acc[2]), "+v"(acc[3]), "+v"(acc[4]), "+v"(acc[5]), "+v"(acc[6]), "+v"(acc[7]));
    {
      const bool hi = (lane & 32) != 0;
      *(LAS f32x4*)(L + oaddr) = f32x4{hi ? acc[4][0] : acc[0][0], hi ? acc[5][0] : acc[1][0], hi ? acc[6][0] : acc[2][0], hi ? acc[7][0] : acc[3][0]};
    }
    lds_arrive(L, 4, lane);
    STAMP(5);
    SCHED();
    if (proj) {
      unsigned done = 0u;
#pragma unroll 1
      for (unsigned spins = 0; spins < (SPIN_CAP << 4) && done != 15u; ++spins) {
        const unsigned fresh = __builtin_amdgcn_readfirstlane(fl[5]) & ~done;
        if (fresh == 0u) { if (AF_SLEEP) __builtin_amdgcn_s_sleep(AF_SLEEP); continue; }
        CBAR();
        STAMP(6);
        const v4u af = *(const LAS v4u*)(L + L_AF + (c >> 2) * 128 + (4 * (c & 1) + g) * 16);
        out_proj(rws, af, wf, tag, ((fresh >> g) & 1u) ? so : OOB, lane);
        done |= fresh;
      }
      STAMP(8);
    }
    FLUSH();
    return;
  }

  __builtin_amdgcn_s_setprio(3);
  const bool qrole = (sub < 12) && (wave < 3);
  const bool cons = idx >= 12 && (idx >> 1) != 13;
  const int ci = cons ? (idx - (idx >= 28 ? 14 : 12)) * 8 + (blk & 7) : 0;
  const int bc = ci / 36, cg = ci % 36;
  const unsigned epoch = __hip_atomic_load((const GAS unsigned*)(p.ws + WS_EPOCH), RLX_AGENT);
  const __amdgpu_buffer_rsrc_t rx = mkrs(p.x + wave * (DM * 2), DM * 2);
  v4u xr = ld16<SM_AUX>(rx, lane * 16);
  unsigned xt = __builtin_amdgcn_raw_buffer_load_b16(rx, 1024 + lane * 2, 0, SM_AUX);
  const int mt = tid & 127;
  v4u m1, mb, m2;
  unsigned m1t, mbt, m2t, tag;
  auto mixer_loads = [&]() {
    const int fo = (blk & 3) * 2304;
    const __amdgpu_buffer_rsrc_t r1 = mkrs(p.w1 + fo, 2304), rb = mkrs(p.b1 + fo, 2304), r2 = mkrs(p.w2 + fo, 2304);
    m1t = __builtin_amdgcn_raw_buffer_load_b16(r1, 2048 + mt * 2, 0, SM_AUX); mbt = __builtin_amdgcn_raw_buffer_load_b16(rb, 2048 + mt * 2, 0, SM_AUX);
    m2t = __builtin_amdgcn_raw_buffer_load_b16(r2, 2048 + mt * 2, 0, SM_AUX);
    m1 = ld16<SM_AUX>(r1, mt * 16); mb = ld16<SM_AUX>(rb, mt * 16); m2 = ld16<SM_AUX>(r2, mt * 16);
    SCHED();
  };
  auto first_barrier = [&]() { SCHED(); LDS_DRAIN(); __builtin_amdgcn_s_barrier(); CBAR(); SCHED(); };
  auto layernorm = [&]() {
    asm volatile("" : "+v"(xr), "+v"(xt));
    tag = __builtin_amdgcn_readfirstlane(epoch) * 0x9E3779B1u + 0x7F4A7C15u;
    f32x2 xv[4];
#pragma unroll
    for (int i = 0; i < 4; ++i) xv[i] = f32x2{bflo(xr[i]), bfhi(xr[i])};
    const float x8 = bflo(xt);
    const f32x2 st = (xv[0] + xv[1]) + (xv[2] + xv[3]);
    float s = st[0] + st[1] + x8;
    s += dpp<XOR1>(s); s += dpp<XOR2>(s); s += dpp<ROR4>(s); s += dpp<ROR8>(s); s = xrow_sum(s);
    const float mean = s * (1.f / DM);
#pragma unroll
    for (int i = 0; i < 4; ++i) xv[i] -= mean;
    const float e8 = x8 - mean;
    const f32x2 qt = (xv[0] * xv[0] + xv[1] * xv[1]) + (xv[2] * xv[2] + xv[3] * xv[3]);
    float q = fmaf(e8, e8, qt[0] + qt[1]);
    q += dpp<XOR1>(q); q += dpp<XOR2>(q); q += dpp<ROR4>(q); q += dpp<ROR8>(q); q = xrow_sum(q);
    const float var = q * (1.f / DM);
    const float rstd = __builtin_amdgcn_rsqf(var + 1e-5f);
    const float hs = rstd * __builtin_amdgcn_rsqf(var * rstd * rstd + 1e-5f);
    LAS unsigned char* row = L + wave * (DM * 2);
    *(LAS v4u*)(row + L_XN + lane * 16) = v4u{cvtpk(xv[0][0] * rstd, xv[0][1] * rstd), cvtpk(xv[1][0] * rstd, xv[1][1] * rstd),
                                              cvtpk(xv[2][0] * rstd, xv[2][1] * rstd), cvtpk(xv[3][0] * rstd, xv[3][1] * rstd)};
    *(LAS unsigned short*)(row + L_XN + 1024 + lane * 2) = (unsigned short)cvtpk(e8 * rstd, 0.f);
    *(LAS v4u*)(row + L_X + lane * 16) = xr;
    *(LAS unsigned short*)(row + L_X + 1024 + lane * 2) = (unsigned short)xt;
    if (lane == 0) { *(LAS f32x2*)(L + L_ST + wave * 8) = f32x2{mean, hs}; fl[3] = tag; }
    lds_arrive(L, 7, lane);
    STAMP(1);
    PF_DONE();
    lds_wait_ge<0>(fl + 7, 4u);
  };
  if (qrole) {
    const int R0 = 192 * (sub >> 2) + 64 * wave, cq = (sub + CQROT) & 3;
    v4u wr[8];
    const __amdgpu_buffer_rsrc_t rq = mkrs(p.wq, WQ_BYTES);
    const int off = (R0 + 8 * (4 * (c >> 3) + g)) * 8192 + (kvh * 256 + 64 * cq) * 2 + (c & 7) * 16;
#pragma unroll
    for (int jj = 0; jj < 8; ++jj) wr[jj] = ld16<WQ_AUX>(rq, off, jj * 8192);
    first_barrier();
    mixer_loads();
    layernorm();
    const v4u af = *(const LAS v4u*)(L + L_XN + (c >> 2) * (DM * 2) + (R0 + 8 * (4 * (c & 1) + g)) * 2);
    f32x4 d[8];
#pragma unroll
    for (int j = 0; j < 8; ++j) d[j] = mfma(af, colfrag(wr, j), f32x4{0.f, 0.f, 0.f, 0.f});
    const bool up = (lane & 8) != 0;
    f32x4 u;
#pragma unroll
    for (int k = 0; k < 4; ++k) { const float keep = up ? d[4 + k][1] : d[k][0], send = up ? d[k][1] : d[4 + k][0]; u[k] = keep + dpp<ROR8>(send); }
    *(LAS f32x4*)(L + L_QP + wave * 1024 + g * 256 + (c & 7) * 32 + (c >> 3) * 16) = u;
    LDS_DRAIN();
    unsigned old = 0u;
    if (lane == 0) old = __hip_atomic_fetch_add((LAS unsigned*)(L + L_FL), 1u, RLX_WG);
    old = __builtin_amdgcn_readfirstlane(old);
    CBAR();
    if (old == 2u) {
      const LAS f32x4* sq = (const LAS f32x4*)(L + L_QP + lane * 16);
      const f32x4 v = sq[0] + sq[64] + sq[128];
      st16_sc1(rws, WS_QP + ((kvh * 4 + g) * 3 + (sub >> 2)) * 1024 + cq * 256 + c * 16, pack4(v, tag));
    }
  } else {
    first_barrier();
    mixer_loads();
    layernorm();
  }
  STAMP(2);
  if (wave == 3 && idx == 31) asm volatile("buffer_wbl2 sc0 sc1" ::: "memory");
  __builtin_amdgcn_s_setprio(0);
  {
    const int ch0 = 36 * (blk >> 2) + 18 * (wave >> 1);
    const f32x2 stt = *(const LAS f32x2*)(L + L_ST + (blk >> 6) * 8);
    const unsigned xh = *(const LAS unsigned*)(L + L_X + (ch0 + 2 * (c < 9 ? c : 0)) * 2);
    const float h0 = (bflo(xh) - stt[0]) * stt[1], h1 = (bfhi(xh) - stt[0]) * stt[1];
    float w1[9], bb[9], w2[9];
#pragma unroll
    for (int i = 0; i < 4; ++i) {
      w1[2 * i] = bflo(m1[i]); w1[2 * i + 1] = bfhi(m1[i]); bb[2 * i] = bflo(mb[i]); bb[2 * i + 1] = bfhi(mb[i]); w2[2 * i] = bflo(m2[i]); w2[2 * i + 1] = bfhi(m2[i]);
    }
    w1[8] = bflo(m1t); bb[8] = bflo(mbt); w2[8] = bflo(m2t);
    constexpr float C1 = -2.0f * 0.7978845608028654f * 1.4426950408889634f, C2 = C1 * 0.044715f;
    float ykeep = 0.f;
#pragma unroll 1
    for (int it = 0; it < 9; ++it) {
      const f32x2 h = {__builtin_bit_cast(float, __builtin_amdgcn_readlane(asu(h0), it)), __builtin_bit_cast(float, __builtin_amdgcn_readlane(asu(h1), it))};
      f32x2 acc = {0.f, 0.f};
#pragma unroll
      for (int i = 0; i < 9; ++i) {
        const f32x2 u = h * w1[i] + bb[i];
        const f32x2 z = u * (u * u * C2 + C1);
        const f32x2 d = f32x2{__builtin_amdgcn_exp2f(z[0]), __builtin_amdgcn_exp2f(z[1])} + 1.0f;
        const f32x2 r = {__builtin_amdgcn_rcpf(d[0]), __builtin_amdgcn_rcpf(d[1])};
        acc = (u * w2[i]) * r + acc;
      }
      const auto sw = __builtin_amdgcn_permlane32_swap(asu(acc[0]), asu(acc[1]), false, false);
      float y = asf(sw[0]) + asf(sw[1]);
      y += dpp<XOR1>(y); y += dpp<XOR2>(y); y += dpp<ROR4>(y); y += dpp<ROR8>(y);
      ykeep = (c == it) ? y : ykeep;
    }
    __builtin_amdgcn_s_setprio(3);
    const auto sy = __builtin_amdgcn_permlane16_swap(asu(ykeep), asu(ykeep), false, false);
    ykeep = asf(sy[0]) + asf(sy[1]);
    if (c < 9 && (g & 1) == 0) *(LAS float*)(L + L_MIX + (wave & 1) * 256 + (18 * (wave >> 1) + 2 * c + (g >> 1)) * 4) = ykeep;
    LDS_DRAIN();
    unsigned old = 0u;
    if (lane == 0) old = __hip_atomic_fetch_add((LAS unsigned*)(L + L_FL + 24), 1u, RLX_WG);
    old = __builtin_amdgcn_readfirstlane(old);
    CBAR();
    if (old == 3u) {
      const LAS f32x4* sy4 = (const LAS f32x4*)(L + L_MIX + c * 16);
      st16_sc1(rws, lane < 9 ? WS_YP + ((blk & 3) * 2304 + 36 * (blk >> 2)) * 4 + lane * 16 : OOB, pack4(sy4[0] + sy4[16], tag));
    }
  }
  const int chn = 4 * (lane & 3) + 2 * (g & 1) + (g >> 1);
  float gt = 0.f, xo = 0.f;
  if (wave == 3 && cons) {
    const unsigned b2v = __builtin_amdgcn_raw_buffer_load_b16(mkrs(p.b2, 2), 0, 0, 0);
    const int yo = WS_YP + ((c >> 2) * 2304 + 16 * ci) * 4 + (lane & 3) * 16;
    v4u yv;
    bool ok = false;
#pragma unroll 1
    for (unsigned spins = 0; spins < SPIN_CAP; ++spins) {
      __builtin_amdgcn_s_sleep(YP_SLEEP);
      CBAR();
      yv = ld16<AUX_SC1>(rws, yo);
      if (__all(tagged(yv, tag))) { ok = true; break; }
    }
    const f32x4 y4 = unpack4(yv);
    float yy = (g & 1) ? ((g >> 1) ? y4[3] : y4[2]) : ((g >> 1) ? y4[1] : y4[0]);
    yy += dpp<ROR4>(yy); yy += dpp<ROR8>(yy);
    yy += bflo(b2v);
    gt = ok ? yy * __builtin_amdgcn_rcpf(1.f + __builtin_amdgcn_exp2f(-1.4426950408889634f * yy)) : __builtin_nanf("");
    xo = bflo(*(const LAS unsigned short*)(L + L_X + (16 * ci + chn) * 2));
  }
  STAMP(3);
  __builtin_amdgcn_s_setprio(3);
  lds_wait_ge<1>(fl + 2, 8u);
  const int half = c & 1, ph = (c >> 1) & 1;
  const int gb = (b + wave) & 3;
  const int base = WS_OP + ((gb * 16 + kvh) * 4 + (c >> 2)) * OP_SRC + (REPL ? b * OP_CPY : 0);
  int o1 = base + 1024, o0 = base + (64 * split + 8 * (4 * half + g) + 4 * ph) * 4;
  if (wave == 0) {
    const f32x2 ml = *(const LAS f32x2*)(L + L_ML + (lane >> 5) * 64 + (lane & 7) * 8);
    float M = fmaxf(ml[0], dpp<XOR1>(ml[0])); M = fmaxf(M, dpp<XOR2>(M)); M = fmaxf(M, dpp<XOR7>(M));
    M = q24(M);
    const float w = __builtin_amdgcn_exp2f(ml[0] - M);
    float Ls = w * ml[1]; Ls += dpp<XOR1>(Ls); Ls += dpp<XOR2>(Ls); Ls += dpp<XOR7>(Ls);
    float ww[8];
    ww[0] = dpp<0x150>(w); ww[1] = dpp<0x151>(w); ww[2] = dpp<0x152>(w); ww[3] = dpp<0x153>(w);
    ww[4] = dpp<0x154>(w); ww[5] = dpp<0x155>(w); ww[6] = dpp<0x156>(w); ww[7] = dpp<0x157>(w);
    const int ob = WS_OP + (pair * 4 + split) * OP_SRC;
    const float M1 = __builtin_bit_cast(float, __builtin_amdgcn_readlane(asu(M), 32)), L1 = __builtin_bit_cast(float, __builtin_amdgcn_readlane(asu(Ls), 32));
    st16_sc1(rws, lane < (REPL ? 4 : 1) ? ob + lane * OP_CPY + 1024 : OOB, pack4(f32x4{M, Ls, M1, L1}, tag));
    int oo = ob + lane * 16;
    asm volatile("" : "+v"(oo), "+v"(ww[0]), "+v"(ww[1]), "+v"(ww[2]), "+v"(ww[3]), "+v"(ww[4]), "+v"(ww[5]), "+v"(ww[6]), "+v"(ww[7]));
    STAMP(4);
    lds_wait_ge<0>(fl + 4, 8u);
    STAMP(5);
    const LAS f32x4* o = (const LAS f32x4*)(L + L_O + lane * 16);
    f32x4 t = o[0] * ww[0];
#pragma unroll
    for (int i = 1; i < 8; ++i) t += o[i * 64] * ww[i];
    const v4u tp = pack4(t, tag);
#pragma unroll
    for (int k = 0; k < (REPL ? 4 : 1); ++k) __builtin_amdgcn_raw_buffer_store_b128(tp, rws, oo, k * OP_CPY, AUX_SC1);
    STAMP(6);
  }
  {
    const bool hd = (split >> 1) != 0;
    float m = 0.f, l = 0.f, sc = 0.f;
    f32x4 x = {0.f, 0.f, 0.f, 0.f};
    bool ok = false, have_sc = false;
#pragma unroll 1
    for (unsigned spins = 0; spins < SPIN_CAP; ++spins) {
      CBAR();
      const int oe = (GATE1 && o1 != OOB && spins != 0) ? OOB : o0;
      const v4u a0 = ld16<AUX_SC1>(rws, oe), ml = ld16<AUX_SC1>(rws, o1);
      const bool hm = (o1 != OOB) & tagged(ml, tag), hx = (oe != OOB) & tagged(a0, tag);
      m = hm ? q24(asf(hd ? ml[2] : ml[0])) : m; l = hm ? q24(asf(hd ? ml[3] : ml[1])) : l; o1 = hm ? OOB : o1;
      x = hx ? unpack4(a0) : x; o0 = hx ? OOB : o0;
      if (!have_sc && __all(o1 == OOB)) {
        float Mg = fmaxf(m, dpp<ROR4>(m)); Mg = fmaxf(Mg, dpp<ROR8>(Mg));
        const float w = __builtin_amdgcn_exp2f(m - Mg);
        float Lg = w * l; Lg += dpp<ROR4>(Lg); Lg += dpp<ROR8>(Lg);
        sc = w * __builtin_amdgcn_rcpf(Lg); have_sc = true;
      }
      if (have_sc && __all(o0 == OOB)) { ok = true; break; }
      if (GATE1 && !__any((o1 == OOB) & (o0 != OOB))) __builtin_amdgcn_s_sleep(GATE1);
    }
    STAMP(7);
    if (!ok) sc = __builtin_nanf("");
    float x0 = x[0] * sc, x1 = x[1] * sc, x2 = x[2] * sc, x3 = x[3] * sc;
    x0 += dpp<ROR4>(x0); x1 += dpp<ROR4>(x1); x2 += dpp<ROR4>(x2); x3 += dpp<ROR4>(x3);
    x0 += dpp<ROR8>(x0); x1 += dpp<ROR8>(x1); x2 += dpp<ROR8>(x2); x3 += dpp<ROR8>(x3);
    if (c < 4) *(LAS v2u*)(L + L_AF + gb * 128 + (4 * half + g) * 16 + ph * 8) = v2u{cvtpk(x0, x1), cvtpk(x2, x3)};
    LDS_DRAIN();
    if (lane == 0) __hip_atomic_fetch_or((LAS unsigned*)(L + L_FL + 20), 1u << gb, RLX_WG);
    STAMP(8);
  }
  if (wave != 3 || !cons) { FLUSH(); return; }
  {
    int so[4];
#pragma unroll
    for (int i = 0; i < 4; ++i)
      so[i] = WS_SP + (16 * i + (lane >> 2)) * 9216 + (cg >> 2) * 1024 + bc * 256 + (cg & 3) * 64 + (lane & 3) * 16;
    v4u sv[4];
#pragma unroll
    for (int i = 0; i < 4; ++i) sv[i] = v4u{0u, 0u, 0u, 0u};
    bool ok = false;
    lds_wait_ge<1>(fl + 5, 15u);
    STAMP(5);
#pragma unroll 1
    for (unsigned spins = 0; spins < SPIN_CAP; ++spins) {
      CBAR();
      v4u sp[4];
#pragma unroll
      for (int i = 0; i < 4; ++i) sp[i] = ld16<AUX_SC1>(rws, so[i]);
      bool good = true;
#pragma unroll
      for (int i = 0; i < 4; ++i) {
        const bool hit = (so[i] != OOB) & tagged(sp[i], tag);
        sv[i] = hit ? sp[i] : sv[i]; so[i] = hit ? OOB : so[i];
        good &= so[i] == OOB;
      }
      if (__all(good)) { ok = true; break; }
      if (GATE2A) {
        int miss = 0;
#pragma unroll
        for (int i = 0; i < 4; ++i) miss += __builtin_popcountll(__ballot(so[i] != OOB));
        if (miss > G2HI) __builtin_amdgcn_s_sleep(GATE2A); else if (miss > G2LO) __builtin_amdgcn_s_sleep(GATE2B);
      }
    }
    STAMP(6);
    f32x4 t = (unpack4(sv[0]) + unpack4(sv[1])) + (unpack4(sv[2]) + unpack4(sv[3]));
    const auto p01 = __builtin_amdgcn_permlane32_swap(asu(t[0]), asu(t[1]), false, false), p23 = __builtin_amdgcn_permlane32_swap(asu(t[2]), asu(t[3]), false, false);
    const auto pq = __builtin_amdgcn_permlane16_swap(asu(asf(p01[0]) + asf(p01[1])), asu(asf(p23[0]) + asf(p23[1])), false, false);
    float r = asf(pq[0]) + asf(pq[1]);
    r += dpp<ROR4>(r); r += dpp<ROR8>(r);
    if (!ok) r = __builtin_nanf("");
    if ((lane & 12) == 0) *(unsigned short*)(p.out + (bc * DM + 16 * cg + chn) * 2) = (unsigned short)cvtpk(fmaf(r, gt, xo), 0.f);
    if (ci == 0 && lane == 0) __hip_atomic_store((GAS unsigned*)(p.ws + WS_EPOCH), tag, RLX_AGENT);
    STAMP(9);
    FLUSH();
  }
}
}

extern "C" void kernel_launch(void* const* d_in, const int* in_sizes, int n_in, void* d_out, int out_size, void* d_ws, size_t ws_size,
                              hipStream_t stream) {
  Args a;
  a.x = (const unsigned char*)d_in[0]; a.k = (const unsigned char*)d_in[1]; a.v = (const unsigned char*)d_in[2];
  a.wq = (const unsigned char*)d_in[3]; a.wo = (const unsigned char*)d_in[4];
  a.w1 = (const unsigned char*)d_in[6]; a.b1 = (const unsigned char*)d_in[7]; a.w2 = (const unsigned char*)d_in[8]; a.b2 = (const unsigned char*)d_in[9];
  a.out = (unsigned char*)d_out; a.ws = (unsigned char*)d_ws;
  hipLaunchKernelGGL(fused_kernel, dim3(256), dim3(NTHR), 0, stream, a);
}
```

```cpp
#include <hip/hip_runtime.h>
#include <hip/hip_bf16.h>
#include <stdint.h>

typedef unsigned v4u __attribute__((ext_vector_type(4)));
typedef unsigned v2u __attribute__((ext_vector_type(2)));
typedef float f32x4 __attribute__((ext_vector_type(4)));
typedef float f32x2 __attribute__((ext_vector_type(2)));
typedef short bf16x8 __attribute__((ext_vector_type(8)));
#define LAS __attribute__((address_space(3)))
#define GAS __attribute__((address_space(1)))
#define RLX_AGENT __ATOMIC_RELAXED, __HIP_MEMORY_SCOPE_AGENT
#define RLX_WG __ATOMIC_RELAXED, __HIP_MEMORY_SCOPE_WORKGROUP

#define T_FIRST 140
#define TAU4 118
#define WINDOW 8
#define Q_AT 330
#define ML_SLEEP 24
#define O_DELAY 4
#define S_DELAY 12
#define GSYNC 1
#define LOOK2 2
#define T_VIS 120
#define T_MARGIN 10
#define TAU_MIN 27
#define TAU_MAX 36
#define RS1 4
#define RS2 8
#define RS4 17
#define RS3 12
#define PTOUCH 1
#define TORG 1
#define CODE_PF 18944

namespace {
constexpr int DM = 576, SK = 8192, WIN = 2048;
constexpr int NTHR = 768;
constexpr int AUX_NT = 2, AUX_SC1 = 16, RSRC_W3 = 0x00020000;
constexpr int OOB = 0x7ffffff0;
constexpr unsigned SPIN_CAP = 1u << 17;
constexpr int WS_EPOCH = 0;
constexpr int WS_TB = 4096;
constexpr int WS_QP = WS_TB + 32768;
constexpr int OP_SRC = 1152;
constexpr int WS_OP = WS_QP + 16 * 4 * 3 * 1024;
constexpr int WS_SP = WS_OP + 64 * 4 * OP_SRC;
constexpr int WS_YP = WS_SP + 64 * 9216;
constexpr int WS_END = WS_YP + 256 * 256;
constexpr size_t WS_STAMP = (size_t)64 << 20;
constexpr int L_K = 0;
constexpr int L_O = 131072;
constexpr int L_QP = L_O + 8192;
constexpr int L_Q = L_QP + 3072;
constexpr int L_ML = L_Q + 512;
constexpr int L_MIX = L_ML + 128;
constexpr int L_X = L_MIX + 512;
constexpr int L_XN = L_X + 4608;
constexpr int L_ST = L_XN + 4608;
constexpr int L_FL = L_ST + 32;
constexpr int L_BYTES = L_FL + 64;
enum { F_QP = 0, F_Q = 1, F_ML = 2, F_WF = 3, F_O = 4, F_GATH = 5, F_MIX = 6, F_LN = 7, F_END = 8 };

struct Args {
  const unsigned char* x; const unsigned char* k; const unsigned char* v; const unsigned char* wq; const unsigned char* wo;
  const unsigned char* w1; const unsigned char* b1; const unsigned char* w2; const unsigned char* b2;
  unsigned char* out; unsigned char* ws;
};

__device__ __forceinline__ __amdgpu_buffer_rsrc_t mkrs(const void* p, int bytes) { return __builtin_amdgcn_make_buffer_rsrc((void*)p, 0, bytes, RSRC_W3); }
template <int AUX> __device__ __forceinline__ v4u ld16(__amdgpu_buffer_rsrc_t rs, int voff, int soff = 0) { return __builtin_amdgcn_raw_buffer_load_b128(rs, voff, soff, AUX); }
template <int AUX> __device__ __forceinline__ unsigned ld4(__amdgpu_buffer_rsrc_t rs, int voff) { return __builtin_amdgcn_raw_buffer_load_b32(rs, voff, 0, AUX); }
__device__ __forceinline__ unsigned ld2(__amdgpu_buffer_rsrc_t rs, int voff) { return __builtin_amdgcn_raw_buffer_load_b16(rs, voff, 0, 0); }
__device__ __forceinline__ void st16_sc1(__amdgpu_buffer_rsrc_t rs, int voff, v4u v) { __builtin_amdgcn_raw_buffer_store_b128(v, rs, voff, 0, AUX_SC1); }
__device__ __forceinline__ float asf(unsigned u) { return __builtin_bit_cast(float, u); }
__device__ __forceinline__ unsigned asu(float f) { return __builtin_bit_cast(unsigned, f); }
__device__ __forceinline__ float bflo(unsigned u) { return asf(u << 16); }
__device__ __forceinline__ float bfhi(unsigned u) { return asf(u & 0xffff0000u); }
__device__ __forceinline__ float rdlane(float x, int l) { return asf((unsigned)__builtin_amdgcn_readlane((int)asu(x), l)); }
__device__ __forceinline__ unsigned cvtpk(float lo, float hi) {
  typedef __bf16 b2 __attribute__((ext_vector_type(2)));
  b2 r = {(__bf16)lo, (__bf16)hi};
  return __builtin_bit_cast(unsigned, r);
}
template <int CTRL> __device__ __forceinline__ float dpp(float x) {
  return __builtin_bit_cast(float, __builtin_amdgcn_mov_dpp(__builtin_bit_cast(int, x), CTRL, 0xf, 0xf, true));
}
template <int CTRL> __device__ __forceinline__ unsigned dppu(unsigned x) { return (unsigned)__builtin_amdgcn_mov_dpp((int)x, CTRL, 0xf, 0xf, true); }
constexpr int XOR1 = 0xB1, XOR2 = 0x4E, XOR7 = 0x141, ROR8 = 0x128, ROR4 = 0x124, BCAST = 0x150;
__device__ __forceinline__ float xrow_sum(float x) {
  auto s = __builtin_amdgcn_permlane16_swap(asu(x), asu(x), false, false);
  x = asf(s[0]) + asf(s[1]);
  auto t = __builtin_amdgcn_permlane32_swap(asu(x), asu(x), false, false);
  return asf(t[0]) + asf(t[1]);
}
__device__ __forceinline__ float xrow_max(float x) {
  auto s = __builtin_amdgcn_permlane16_swap(asu(x), asu(x), false, false);
  x = fmaxf(asf(s[0]), asf(s[1]));
  auto t = __builtin_amdgcn_permlane32_swap(asu(x), asu(x), false, false);
  return fmaxf(asf(t[0]), asf(t[1]));
}
__device__ __forceinline__ f32x4 mfma(v4u a, v4u b, f32x4 c) {
  return __builtin_amdgcn_mfma_f32_16x16x32_bf16(__builtin_bit_cast(bf16x8, a), __builtin_bit_cast(bf16x8, b), c, 0, 0, 0);
}
__device__ __forceinline__ v4u colfrag(const v4u* r, int j) {
  const unsigned sel = (j & 1) ? 0x07060302u : 0x05040100u;
  v4u f;
  f[0] = __builtin_amdgcn_perm(r[1][j >> 1], r[0][j >> 1], sel);
  f[1] = __builtin_amdgcn_perm(r[3][j >> 1], r[2][j >> 1], sel);
  f[2] = __builtin_amdgcn_perm(r[5][j >> 1], r[4][j >> 1], sel);
  f[3] = __builtin_amdgcn_perm(r[7][j >> 1], r[6][j >> 1], sel);
  return f;
}
#define CBAR() asm volatile("" ::: "memory")
#define LDS_DRAIN() asm volatile("s_waitcnt lgkmcnt(0)" ::: "memory")
#define SCHED() __builtin_amdgcn_sched_barrier(0)
#define VM_WAIT(n) do { SCHED(); asm volatile("s_waitcnt vmcnt(%0)" :: "n"(n) : "memory"); SCHED(); } while (0)
#define NOW() ((unsigned)__builtin_amdgcn_s_memrealtime())
#define STAMP(i) do { } while (0)
#define FLUSH() do { } while (0)

template <int SLEEP> __device__ __forceinline__ void lds_wait_ge(volatile LAS unsigned* w, unsigned want) {
#pragma unroll 1
  for (unsigned spins = 0; spins < (SPIN_CAP << 4); ++spins) {
    if (__builtin_amdgcn_readfirstlane(*w) >= want) break;
    if (SLEEP) __builtin_amdgcn_s_sleep(SLEEP);
  }
  CBAR();
}
__device__ __forceinline__ void lds_arrive(LAS unsigned char* L, int word, int lane) {
  LDS_DRAIN();
  if (lane == 0) __hip_atomic_fetch_add((LAS unsigned*)(L + L_FL + 4 * word), 1u, RLX_WG);
}
__device__ __forceinline__ unsigned lds_ticket(LAS unsigned char* L, int word, int lane) {
  LDS_DRAIN();
  unsigned old = 0u;
  if (lane == 0) old = __hip_atomic_fetch_add((LAS unsigned*)(L + L_FL + 4 * word), 1u, RLX_WG);
  old = __builtin_amdgcn_readfirstlane(old);
  CBAR();
  return old;
}
__device__ __forceinline__ v4u pack4(f32x4 x, unsigned tag) {
  return v4u{__builtin_amdgcn_perm(asu(x[0]), tag, 0x07060500u), __builtin_amdgcn_perm(asu(x[1]), tag, 0x07060501u),
             __builtin_amdgcn_perm(asu(x[2]), tag, 0x07060502u), __builtin_amdgcn_perm(asu(x[3]), tag, 0x07060503u)};
}
__device__ __forceinline__ float q24(float x) { return asf(asu(x) & 0xffffff00u); }
__device__ __forceinline__ f32x4 unpack4(v4u v) { return f32x4{asf(v[0] & 0xffffff00u), asf(v[1] & 0xffffff00u), asf(v[2] & 0xffffff00u), asf(v[3] & 0xffffff00u)}; }
__device__ __forceinline__ bool tagged(v4u v, unsigned tag) {
  const unsigned lo = __builtin_amdgcn_perm(v[1], v[0], 0x0c0c0400u), hi = __builtin_amdgcn_perm(v[3], v[2], 0x04000c0cu);
  return (lo | hi) == tag;
}
template <int N, int STRIDE, int SLEEP> __device__ __forceinline__ f32x4 gather_sum(__amdgpu_buffer_rsrc_t rws, int off, unsigned tag) {
  v4u t[N];
  bool good;
  unsigned spins = 0;
#pragma unroll 1
  do {
    if (SLEEP && spins) __builtin_amdgcn_s_sleep(SLEEP);
    CBAR();
#pragma unroll
    for (int i = 0; i < N; ++i) t[i] = ld16<AUX_SC1>(rws, off, i * STRIDE);
    good = true;
#pragma unroll
    for (int i = 0; i < N; ++i) good &= tagged(t[i], tag);
  } while (!good && ++spins < SPIN_CAP);
  f32x4 s = unpack4(t[0]);
  if (N == 4) s = (s + unpack4(t[1])) + (unpack4(t[2]) + unpack4(t[3]));
  else {
#pragma unroll
    for (int i = 1; i < N; ++i) s += unpack4(t[i]);
  }
  if (!good) s[0] = __builtin_nanf("");
  return s;
}
template <bool MM, bool EP> __device__ __forceinline__ f32x4 project_step(f32x4 (&d)[8], v4u af, const v4u (&wf)[8], f32x4 (&e)[8], int lane) {
  const bool up = (lane & 8) != 0;
  f32x4 u = {0.f, 0.f, 0.f, 0.f};
#pragma unroll
  for (int k = 0; k < 4; ++k) {
    float keep = 0.f, send = 0.f;
    if (MM) { d[k] = mfma(af, wf[k], f32x4{0.f, 0.f, 0.f, 0.f}); SCHED(); }
    if (EP) { asm volatile("" : "+v"(e[k]), "+v"(e[4 + k])); keep = up ? e[4 + k][1] : e[k][0]; send = up ? e[k][1] : e[4 + k][0]; SCHED(); }
    if (MM) { d[4 + k] = mfma(af, wf[4 + k], f32x4{0.f, 0.f, 0.f, 0.f}); SCHED(); }
    if (EP) { u[k] = keep + dpp<ROR8>(send); SCHED(); }
  }
  return u;
}

__global__ void __launch_bounds__(NTHR, 3) fused_kernel(Args p) {
  __shared__ __attribute__((aligned(16))) unsigned char smem[L_BYTES];
  LAS unsigned char* L = (LAS unsigned char*)smem;
  volatile LAS unsigned* fl = (volatile LAS unsigned*)(L + L_FL);
  const int tid = threadIdx.x, lane = tid & 63, wave = __builtin_amdgcn_readfirstlane(tid >> 6);
  const int c = lane & 15, g = lane >> 4;
  const int blk = blockIdx.x, idx = blk >> 3;
  const int kvh = 2 * (blk & 7) + (idx >> 4), sub = idx & 15, b = sub >> 2, split = sub & 3, pair = b * 16 + kvh;
  const __amdgpu_buffer_rsrc_t rws = mkrs(p.ws, WS_END);
  STAMP(0);
  const unsigned t00 = NOW();
  unsigned tstart = NOW();
  const unsigned pf = ld4<0>(mkrs((const void*)(__builtin_amdgcn_s_getpc() & ~255ull), CODE_PF), wave >= 7 ? (tid - 448) * 64 : OOB);
  unsigned tch;
  {
    const int sh = 4 * (idx & 15);
    const unsigned long long base = ((const __attribute__((address_space(4))) unsigned long long*)__builtin_amdgcn_kernarg_segment_ptr())
        [(((idx & 16) ? 0x2121AA2198217621ull : 0x5021442133213321ull) >> sh) & 15u];
    const unsigned kind = (unsigned)(((idx & 16) ? 0x9911801100110011ull : 0x0011A21164114011ull) >> sh);
    unsigned off = (kind & 1u) * ((unsigned)(pair * SK + (SK - WIN)) * 256u) + ((kind >> 3) & 1u) * 524286u;
    off += ((kind & 6u) == 2u ? (blk & 7) * 512u * (DM * 2u) : 0u) + ((kind & 6u) == 4u ? (2u << 20) << (idx >> 2) : 0u) + ((kind & 6u) == 6u ? DM * 8192u - 2u : 0u);
    asm volatile("" :: "s"(p.x), "s"(p.w2), "s"(p.b2), "s"(p.out), "s"(p.ws), "s"(base), "s"(off));
    tch = ld2(mkrs((const void*)(base + off), 2), tid == 256 ? 0 : OOB);
  }
  SCHED();
  if (tid < 16) fl[tid] = tid == F_END ? tstart + T_FIRST + 31 * TAU4 / 4 : 0u;
  LDS_DRAIN();
  __builtin_amdgcn_s_barrier();
  CBAR();
  tstart = NOW();
  if (tid == 0) fl[F_END] = tstart + T_FIRST + 31 * TAU4 / 4;
  const int ngrp = b == 3 ? 3 : 2;

  if (wave >= 4) {
    const int a = wave - 4;
    const size_t kvoff = ((size_t)pair * SK + (SK - WIN) + split * 512 + a * 64) * 256;
    const __amdgpu_buffer_rsrc_t rk = mkrs(p.k + kvoff, 16384), rv = mkrs(p.v + kvoff, 16384);
    const unsigned ldsk = (unsigned)(uintptr_t)(L + L_K) + a * 16384;
    const int hr = ((idx + 2 * a) & 15) << 10;
    int kv[4];
#pragma unroll
    for (int r = 0; r < 4; ++r) kv[r] = g * 256 + ((c ^ (4 * g + r)) << 4);
    __builtin_amdgcn_s_setprio(2);
    v4u wr[8];
    const bool proj = a < ngrp;
    if (proj) {
      const __amdgpu_buffer_rsrc_t ro = mkrs(p.wo, 4096 * DM * 2);
      const int wo_off = (kvh * 256 + 64 * split + 8 * (4 * (c >> 3) + g)) * (DM * 2) + (2 * b + a) * 128 + (c & 7) * 16;
#pragma unroll
      for (int jj = 0; jj < 8; ++jj) wr[jj] = ld16<AUX_NT>(ro, wo_off, jj * (DM * 2));
    } else {
#pragma unroll
      for (int jj = 0; jj < 8; ++jj) wr[jj] = v4u{0u, 0u, 0u, 0u};
    }
    STAMP(4);
    unsigned tref = tstart + T_FIRST;
    int step = TAU4 * 4;
#define RESPREAD(n) do { const unsigned at_ = tref + ((n) * step >> 4); \
      int s_ = ((int)(__builtin_amdgcn_readfirstlane(fl[F_END]) - at_) * (65536 / (31 - (n)))) >> 12; \
      step = s_ < TAU_MIN * 16 ? TAU_MIN * 16 : (s_ > TAU_MAX * 16 ? TAU_MAX * 16 : s_); tref = at_ - ((n) * step >> 4); } while (0)
#define SLOT(n) do { SCHED(); if ((n) == RS1 || (n) == RS2 || (n) == RS3 || (n) == RS4) RESPREAD((n) - 1); \
      while ((int)(NOW() - tref) < ((n) * step >> 4)) __builtin_amdgcn_s_sleep(1); SCHED(); } while (0)
#define V_AT(i, n) do { SLOT(n); vr[i] = ld16<AUX_NT>(rv, lane * 16, ((i) << 10) ^ hr); VM_WAIT(WINDOW); } while (0)
#define V_PIECE(i) V_AT(i, 16 + (i))
#pragma unroll
    for (int i = 0; i < 16; ++i) {
      SLOT(i);
      unsigned keep;
      asm volatile("s_mov_b32 %0, m0\n\ts_mov_b32 m0, %1\n\ts_nop 0\n\tbuffer_load_dwordx4 %2, %3, %4 offen nt lds\n\ts_mov_b32 m0, %0"
                   : "=&s"(keep) : "s"(ldsk + i * 1024), "v"(kv[i & 3]), "s"(rk), "s"((i << 10) ^ hr) : "memory");
      VM_WAIT(i < 8 ? WINDOW + 8 : WINDOW);
    }
    STAMP(9);
    v4u vr[16];
#pragma unroll
    for (int i = 0; i < 5; ++i) V_PIECE(i);
    V_AT(5, 20);
    asm volatile("" :: "v"(pf));
    asm volatile("" :: "v"(tch));
    STAMP(1);
    lds_wait_ge<1>(fl + F_Q, 1u);
    v4u qf[4];
#pragma unroll
    for (int kk = 0; kk < 4; ++kk) qf[kk] = *(const LAS v4u*)(L + L_Q + ((c >> 2) & 1) * 256 + kk * 64 + g * 16);
    STAMP(2);
    VM_WAIT(6);
    f32x4 S[4];
    {
      const LAS unsigned char* kb = L + L_K + a * 16384 + (c & 3) * 1024 + (c >> 2) * 256;
#pragma unroll
      for (int t = 0; t < 4; ++t) S[t] = f32x4{0.f, 0.f, 0.f, 0.f};
#pragma unroll
      for (int kk = 0; kk < 4; ++kk) {
        v4u kf[4];
#pragma unroll
        for (int t = 0; t < 4; ++t) kf[t] = *(const LAS v4u*)(kb + t * 4096 + (((4 * kk) ^ g ^ c) << 4));
#pragma unroll
        for (int t = 0; t < 4; ++t) S[t] = mfma(kf[t], qf[kk], S[t]);
      }
    }
    asm volatile("" : "+v"(S[0]), "+v"(S[1]), "+v"(S[2]), "+v"(S[3]));
    V_PIECE(6);
    constexpr float SC = 0.088388347648318440f * 1.4426950408889634f;
    float mx = -INFINITY;
#pragma unroll
    for (int t = 0; t < 4; ++t) mx = fmaxf(mx, fmaxf(fmaxf(S[t][0], S[t][1]), fmaxf(S[t][2], S[t][3])));
    mx = xrow_max(mx) * SC;
    float ls = 0.f;
    v4u pr[2];
#pragma unroll
    for (int t = 0; t < 4; ++t) {
      float e[4];
#pragma unroll
      for (int r = 0; r < 4; ++r) e[r] = __builtin_amdgcn_exp2f(fmaf(S[t][r], SC, -mx));
      ls += (e[0] + e[1]) + (e[2] + e[3]);
      pr[t >> 1][2 * (t & 1)] = cvtpk(e[0], e[1]);
      pr[t >> 1][2 * (t & 1) + 1] = cvtpk(e[2], e[3]);
    }
    ls = xrow_sum(ls);
    if ((lane & 0x3b) == 0) *(LAS f32x2*)(L + L_ML + (lane >> 2) * 64 + a * 8) = f32x2{mx, ls};
    lds_arrive(L, F_ML, lane);
    asm volatile("" : "+v"(pr[0]), "+v"(pr[1]));
    STAMP(3);
    V_PIECE(7);
    if (proj) {
#pragma unroll
      for (int j = 0; j < 8; ++j) *(LAS v4u*)(L + L_K + a * 16384 + j * 1024 + lane * 16) = colfrag(wr, j);
      lds_arrive(L, F_WF, lane);
    }
    unsigned oaddr = L_O + a * 1024 + (g & 1) * 512 + c * 32 + (g >> 1) * 16;
    asm volatile("" : "+v"(oaddr));
    V_PIECE(8);
    V_PIECE(9);
    V_PIECE(10);
    f32x4 acc[8];
#pragma unroll
    for (int j = 0; j < 8; ++j) { acc[j] = mfma(pr[0], colfrag(vr, j), f32x4{0.f, 0.f, 0.f, 0.f}); asm volatile("" : "+v"(acc[j])); }
    v4u vf[8];
#define V_PAIR(d) do { _Pragma("unroll") for (int j = 0; j < 8; ++j) { \
      unsigned t_ = __builtin_amdgcn_perm(vr[9 + 2 * (d)][j >> 1], vr[8 + 2 * (d)][j >> 1], (j & 1) ? 0x07060302u : 0x05040100u); \
      asm volatile("" : "+v"(t_)); vf[j][d] = t_; } SCHED(); } while (0)
    V_PIECE(11);
    V_PIECE(12);
    V_PAIR(0);
    V_PIECE(13);
    V_PIECE(14);
    V_PAIR(1);
    V_PIECE(15);
    STAMP(7);
    V_PAIR(2);
    V_PAIR(3);
#pragma unroll
    for (int j = 0; j < 8; ++j) acc[j] = mfma(pr[1], vf[j], acc[j]);
    asm volatile("" : "+v"(acc[0]), "+v"(acc[1]), "+v"(acc[2]), "+v"(acc[3]), "+v"(acc[4]), "+v"(acc[5]), "+v"(acc[6]), "+v"(acc[7]));
    {
      const bool hi = (lane & 32) != 0;
      *(LAS f32x4*)(L + oaddr) = f32x4{hi ? acc[4][0] : acc[0][0], hi ? acc[5][0] : acc[1][0], hi ? acc[6][0] : acc[2][0], hi ? acc[7][0] : acc[3][0]};
    }
    lds_arrive(L, F_O, lane);
    STAMP(5);
    FLUSH();
    return;
  }

  __builtin_amdgcn_s_setprio(3);
  const bool qrole = (sub < 12) && (wave < 3);
  const unsigned epoch = ld4<0>(rws, WS_EPOCH);
  const __amdgpu_buffer_rsrc_t rx = mkrs(p.x + wave * (DM * 2), DM * 2);
  v4u xr = ld16<0>(rx, lane * 16);
  unsigned xt = ld2(rx, 1024 + lane * 2);
  const int mt = tid & 127;
  v4u m1, mb, m2;
  unsigned m1t, mbt, m2t, tag, warm;
  auto more_loads = [&]() {
    SCHED();
    const int fo = (blk & 3) * 2304;
    const __amdgpu_buffer_rsrc_t r1 = mkrs(p.w1 + fo, 2304), rb = mkrs(p.b1 + fo, 2304), r2 = mkrs(p.w2 + fo, 2304);
    m1t = ld2(r1, 2048 + mt * 2); mbt = ld2(rb, 2048 + mt * 2); m2t = ld2(r2, 2048 + mt * 2);
    m1 = ld16<0>(r1, mt * 16); mb = ld16<0>(rb, mt * 16); m2 = ld16<0>(r2, mt * 16);
    warm = ld4<AUX_SC1>(rws, (wave == 3 && lane < 9) ? WS_OP + (pair * 4 + split) * OP_SRC + lane * 128 : OOB);
    SCHED();
  };
  auto layernorm = [&]() {
    asm volatile("" : "+v"(xr), "+v"(xt));
    tag = __builtin_amdgcn_readfirstlane(epoch) * 0x9E3779B1u + 0x7F4A7C15u;
    if (GSYNC && wave == 3 && idx == 0) st16_sc1(rws, lane < 32 ? WS_TB + blk * 4096 + lane * 128 : OOB, pack4(f32x4{asf(tstart << 8), 0.f, 0.f, 0.f}, tag));
    f32x2 xv[4];
#pragma unroll
    for (int i = 0; i < 4; ++i) xv[i] = f32x2{bflo(xr[i]), bfhi(xr[i])};
    const float x8 = bflo(xt);
    const f32x2 st = (xv[0] + xv[1]) + (xv[2] + xv[3]);
    float s = st[0] + st[1] + x8;
    s += dpp<XOR1>(s); s += dpp<XOR2>(s); s += dpp<ROR4>(s); s += dpp<ROR8>(s); s = xrow_sum(s);
    const float mean = s * (1.f / DM);
#pragma unroll
    for (int i = 0; i < 4; ++i) xv[i] -= mean;
    const float e8 = x8 - mean;
    const f32x2 qt = (xv[0] * xv[0] + xv[1] * xv[1]) + (xv[2] * xv[2] + xv[3] * xv[3]);
    float q = fmaf(e8, e8, qt[0] + qt[1]);
    q += dpp<XOR1>(q); q += dpp<XOR2>(q); q += dpp<ROR4>(q); q += dpp<ROR8>(q); q = xrow_sum(q);
    const float var = q * (1.f / DM);
    const float rstd = __builtin_amdgcn_rsqf(var + 1e-5f);
    const float hs = rstd * __builtin_amdgcn_rsqf(var * rstd * rstd + 1e-5f);
    LAS unsigned char* row = L + wave * (DM * 2);
    *(LAS v4u*)(row + L_XN + lane * 16) = v4u{cvtpk(xv[0][0] * rstd, xv[0][1] * rstd), cvtpk(xv[1][0] * rstd, xv[1][1] * rstd),
                                              cvtpk(xv[2][0] * rstd, xv[2][1] * rstd), cvtpk(xv[3][0] * rstd, xv[3][1] * rstd)};
    *(LAS unsigned short*)(row + L_XN + 1024 + lane * 2) = (unsigned short)cvtpk(e8 * rstd, 0.f);
    *(LAS v4u*)(row + L_X + lane * 16) = xr;
    *(LAS unsigned short*)(row + L_X + 1024 + lane * 2) = (unsigned short)xt;
    if (lane == 0) *(LAS f32x2*)(L + L_ST + wave * 8) = f32x2{mean, hs};
    lds_arrive(L, F_LN, lane);
    STAMP(1);
    asm volatile("" :: "v"(pf));
    asm volatile("" :: "v"(tch));
    lds_wait_ge<0>(fl + F_LN, 4u);
  };
  if (__builtin_expect(qrole, 1)) {
    const int qi = (idx >> 4) * 12 + sub, ks = qi >> 3, G = 8 * (qi & 7) + ((blk + qi) & 7);
    const int R0 = 192 * ks + 64 * wave;
    v4u wr[8];
    const __amdgpu_buffer_rsrc_t rq = mkrs(p.wq, DM * 4096 * 2);
    const int off = (R0 + 8 * (4 * (c >> 3) + g)) * 8192 + G * 128 + (c & 7) * 16;
#pragma unroll
    for (int jj = 0; jj < 8; ++jj) wr[jj] = ld16<AUX_NT>(rq, off, jj * 8192);
    more_loads();
    layernorm();
    const v4u af = *(const LAS v4u*)(L + L_XN + (c >> 2) * (DM * 2) + (R0 + 8 * (4 * (c & 1) + g)) * 2);
    v4u wf[8];
#pragma unroll
    for (int j = 0; j < 8; ++j) wf[j] = colfrag(wr, j);
    f32x4 d[8];
    project_step<true, false>(d, af, wf, d, lane);
    *(LAS f32x4*)(L + L_QP + wave * 1024 + g * 256 + (c & 7) * 32 + (c >> 3) * 16) = project_step<false, true>(d, af, wf, d, lane);
    if (lds_ticket(L, F_QP, lane) == 2u) {
      const LAS f32x4* sq = (const LAS f32x4*)(L + L_QP + lane * 16);
      st16_sc1(rws, WS_QP + (((G >> 2) * 4 + g) * 3 + ks) * 1024 + (G & 3) * 256 + c * 16, pack4(sq[0] + sq[64] + sq[128], tag));
    }
  } else {
    more_loads();
    layernorm();
  }
  STAMP(2);
  __builtin_amdgcn_s_setprio(0);
  {
    const int ch0 = 36 * (blk >> 2) + 18 * (wave >> 1);
    const f32x2 stt = *(const LAS f32x2*)(L + L_ST + (blk >> 6) * 8);
    const unsigned xh = *(const LAS unsigned*)(L + L_X + (ch0 + 2 * (c < 9 ? c : 0)) * 2);
    const float h0 = (bflo(xh) - stt[0]) * stt[1], h1 = (bfhi(xh) - stt[0]) * stt[1];
    float w1[9], bb[9], w2[9];
#pragma unroll
    for (int i = 0; i < 4; ++i) {
      w1[2 * i] = bflo(m1[i]); w1[2 * i + 1] = bfhi(m1[i]); bb[2 * i] = bflo(mb[i]); bb[2 * i + 1] = bfhi(mb[i]); w2[2 * i] = bflo(m2[i]); w2[2 * i + 1] = bfhi(m2[i]);
    }
    w1[8] = bflo(m1t); bb[8] = bflo(mbt); w2[8] = bflo(m2t);
    constexpr float C1 = -2.0f * 0.7978845608028654f * 1.4426950408889634f, C2 = C1 * 0.044715f;
    float ykeep = 0.f;
    bool have_q = wave != (GSYNC ? 1 : 3);
    asm volatile("" :: "v"(warm));
#pragma unroll 1
    for (int it = 0; it < 9; ++it) {
      if (GSYNC && ((wave == 3 && it == 0) || (wave == 2 && it == LOOK2))) {
        CBAR();
        const int guess = (int)(NOW() - tstart) - T_VIS;
        const v4u tb = ld16<AUX_SC1>(rws, lane < 8 ? WS_TB + lane * 4096 + idx * 128 : OOB);
        float d = tagged(tb, tag) ? (float)((int)((tb[0] & 0xffffff00u) - (tstart << 8)) >> 8) : (float)(guess > 0 ? guess : 0);
        d += dpp<XOR1>(d); d += dpp<XOR2>(d); d += dpp<XOR7>(d);
        if (lane == 0) fl[F_END] = tstart + (int)(d * 0.125f) + T_MARGIN + T_FIRST + 31 * TAU4 / 4;
      }
      if (!have_q && (it == 8 || (int)(NOW() + (T_FIRST + 31 * TAU4 / 4 - Q_AT) - __builtin_amdgcn_readfirstlane(fl[F_END])) >= 0)) {
        have_q = true;
        __builtin_amdgcn_s_setprio(3);
        const f32x4 qs = gather_sum<3, 1024, 8>(rws, WS_QP + (kvh * 4 + b) * 3 * 1024 + lane * 16, tag);
        *(LAS v2u*)(L + L_Q + lane * 8) = v2u{cvtpk(qs[0], qs[1]), cvtpk(qs[2], qs[3])};
        lds_arrive(L, F_Q, lane);
        STAMP(4);
        __builtin_amdgcn_s_setprio(0);
      }
      const f32x2 h = {rdlane(h0, it), rdlane(h1, it)};
      f32x2 acc = {0.f, 0.f};
#pragma unroll
      for (int i = 0; i < 9; ++i) {
        const f32x2 u = h * w1[i] + bb[i];
        const f32x2 z = u * (u * u * C2 + C1);
        const f32x2 d = f32x2{__builtin_amdgcn_exp2f(z[0]), __builtin_amdgcn_exp2f(z[1])} + 1.0f;
        const f32x2 r = {__builtin_amdgcn_rcpf(d[0]), __builtin_amdgcn_rcpf(d[1])};
        acc = (u * w2[i]) * r + acc;
      }
      const auto sw = __builtin_amdgcn_permlane32_swap(asu(acc[0]), asu(acc[1]), false, false);
      float y = asf(sw[0]) + asf(sw[1]);
      y += dpp<XOR1>(y); y += dpp<XOR2>(y); y += dpp<ROR4>(y); y += dpp<ROR8>(y);
      ykeep = (c == it) ? y : ykeep;
    }
    __builtin_amdgcn_s_setprio(3);
    const auto sy = __builtin_amdgcn_permlane16_swap(asu(ykeep), asu(ykeep), false, false);
    ykeep = asf(sy[0]) + asf(sy[1]);
    if (c < 9 && (g & 1) == 0) *(LAS float*)(L + L_MIX + (wave & 1) * 256 + (18 * (wave >> 1) + 2 * c + (g >> 1)) * 4) = ykeep;
    if (lds_ticket(L, F_MIX, lane) == 3u) {
      const LAS f32x4* sy4 = (const LAS f32x4*)(L + L_MIX + c * 16);
      st16_sc1(rws, lane < 16 ? WS_YP + blk * 256 + lane * 16 : OOB, pack4(sy4[0] + sy4[16], tag));
    }
  }
  STAMP(3);

  if (wave == 0) {
    lds_wait_ge<1>(fl + F_ML, 8u);
    const f32x2 ml = *(const LAS f32x2*)(L + L_ML + (lane >> 5) * 64 + (lane & 7) * 8);
    float M = fmaxf(ml[0], dpp<XOR1>(ml[0])); M = fmaxf(M, dpp<XOR2>(M)); M = fmaxf(M, dpp<XOR7>(M));
    M = q24(M);
    float Ls = __builtin_amdgcn_exp2f(ml[0] - M) * ml[1];
    Ls += dpp<XOR1>(Ls); Ls += dpp<XOR2>(Ls); Ls += dpp<XOR7>(Ls);
    const int ob = WS_OP + pair * 4 * OP_SRC;
    st16_sc1(rws, lane == 0 ? ob + split * OP_SRC + 1024 : OOB, pack4(f32x4{M, Ls, rdlane(M, 32), rdlane(Ls, 32)}, tag));
    __builtin_amdgcn_s_sleep(ML_SLEEP);
    const f32x4 st4 = gather_sum<1, 0, ML_SLEEP>(rws, ob + (lane & 3) * OP_SRC + 1024, tag);
    const float ms = lane < 32 ? st4[0] : st4[2];
    float Mg = fmaxf(ms, dpp<XOR1>(ms)); Mg = fmaxf(Mg, dpp<XOR2>(Mg));
    float Lg = __builtin_amdgcn_exp2f(ms - Mg) * (lane < 32 ? st4[1] : st4[3]);
    Lg += dpp<XOR1>(Lg); Lg += dpp<XOR2>(Lg);
    const float w = __builtin_amdgcn_exp2f(ml[0] - Mg) * __builtin_amdgcn_rcpf(Lg);
    float ww[8] = {dpp<BCAST>(w), dpp<BCAST + 1>(w), dpp<BCAST + 2>(w), dpp<BCAST + 3>(w), dpp<BCAST + 4>(w), dpp<BCAST + 5>(w), dpp<BCAST + 6>(w), dpp<BCAST + 7>(w)};
    int oo = ob + split * OP_SRC + lane * 16;
    asm volatile("" : "+v"(oo), "+v"(ww[0]), "+v"(ww[1]), "+v"(ww[2]), "+v"(ww[3]), "+v"(ww[4]), "+v"(ww[5]), "+v"(ww[6]), "+v"(ww[7]));
    STAMP(4);
    lds_wait_ge<2>(fl + F_O, 1u);
    lds_wait_ge<0>(fl + F_O, 8u);
    STAMP(5);
    const LAS f32x4* o = (const LAS f32x4*)(L + L_O + lane * 16);
    const f32x4 t = ((o[0] * ww[0] + o[64] * ww[1]) + (o[128] * ww[2] + o[192] * ww[3])) + ((o[256] * ww[4] + o[320] * ww[5]) + (o[384] * ww[6] + o[448] * ww[7]));
    st16_sc1(rws, oo, pack4(t, tag));
    STAMP(6);
  } else if (wave == 2) {
    lds_wait_ge<1>(fl + F_WF, (unsigned)ngrp);
    v4u wf[3][8];
#pragma unroll
    for (int k = 0; k < 3; ++k)
#pragma unroll
      for (int j = 0; j < 8; ++j) { wf[k][j] = *(const LAS v4u*)(L + L_K + k * 16384 + j * 1024 + lane * 16); asm volatile("" : "+v"(wf[k][j])); }
    const bool ph = (c & 2) != 0;
    int oo = WS_OP + ((c >> 2) * 16 + kvh) * 4 * OP_SRC + (64 * split + 8 * (4 * (c & 1) + g) + (c & 2) * 2) * 4;
    int so = WS_SP + (kvh * 4 + split) * 9216 + 2 * b * 1024 + g * 256 + (c & 7) * 32 + (c >> 3) * 16;
    asm volatile("" : "+v"(so), "+v"(oo));
    lds_wait_ge<2>(fl + F_O, 1u);
    {
      const unsigned wm = ld4<AUX_SC1>(rws, lane < 8 * ngrp ? WS_SP + (kvh * 4 + split) * 9216 + 2 * b * 1024 + lane * 128 : OOB);
      asm volatile("" :: "v"(wm));
    }
    lds_wait_ge<0>(fl + F_O, 8u);
    if (O_DELAY) __builtin_amdgcn_s_sleep(O_DELAY);
    STAMP(5);
    const f32x4 x = gather_sum<4, OP_SRC, 0>(rws, oo, tag);
    STAMP(7);
    const unsigned p0 = cvtpk(x[0], x[1]), p1 = cvtpk(x[2], x[3]);
    const unsigned q0 = dppu<XOR2>(p0), q1 = dppu<XOR2>(p1);
    const v4u af = {ph ? q0 : p0, ph ? q1 : p1, ph ? p0 : q0, ph ? p1 : q1};
    f32x4 d0[8], d1[8], d2[8];
    project_step<true, false>(d0, af, wf[0], d0, lane);
    st16_sc1(rws, so, pack4(project_step<true, true>(d1, af, wf[1], d0, lane), tag));
    if (ngrp == 3) {
      st16_sc1(rws, so + 1024, pack4(project_step<true, true>(d2, af, wf[2], d1, lane), tag));
      st16_sc1(rws, so + 2048, pack4(project_step<false, true>(d2, af, wf[2], d2, lane), tag));
    } else {
      st16_sc1(rws, so + 1024, pack4(project_step<false, true>(d1, af, wf[1], d1, lane), tag));
    }
    if (lane == 0) fl[F_GATH] = 1u;
    STAMP(8);
  } else if (wave == 3 && idx >= 12 && (idx >> 1) != 13) {
    const int ci = (idx - (idx >= 28 ? 14 : 12)) * 8 + (blk & 7), bc = ci / 36, cg = ci % 36;
    const int chn = 4 * (lane & 3) + 2 * (g & 1) + (g >> 1);
    const unsigned b2v = ld2(mkrs(p.b2, 2), 0);
    lds_wait_ge<2>(fl + F_ML, 8u);
    const int yu = 4 * ci + (lane & 3);
    const f32x4 y4 = gather_sum<1, 0, 24>(rws, WS_YP + (4 * (yu / 9) + (c >> 2)) * 256 + (yu % 9) * 16, tag);
    float yy = (g & 1) ? ((g >> 1) ? y4[3] : y4[2]) : ((g >> 1) ? y4[1] : y4[0]);
    yy += dpp<ROR4>(yy); yy += dpp<ROR8>(yy);
    yy += bflo(b2v);
    float gt = yy * __builtin_amdgcn_rcpf(1.f + __builtin_amdgcn_exp2f(-1.4426950408889634f * yy));
    float xo = bflo(*(const LAS unsigned short*)(L + L_X + (16 * ci + chn) * 2));
    int so = WS_SP + (lane >> 2) * 4 * 9216 + (cg >> 2) * 1024 + bc * 256 + (cg & 3) * 64 + (lane & 3) * 16;
    asm volatile("" : "+v"(gt), "+v"(xo), "+v"(so));
    STAMP(7);
    lds_wait_ge<1>(fl + F_GATH, 1u);
    if (S_DELAY) __builtin_amdgcn_s_sleep(S_DELAY);
    STAMP(5);
    const f32x4 t = gather_sum<4, 9216, 0>(rws, so, tag);
    STAMP(6);
    const auto p01 = __builtin_amdgcn_permlane32_swap(asu(t[0]), asu(t[1]), false, false), p23 = __builtin_amdgcn_permlane32_swap(asu(t[2]), asu(t[3]), false, false);
    const auto pq = __builtin_amdgcn_permlane16_swap(asu(asf(p01[0]) + asf(p01[1])), asu(asf(p23[0]) + asf(p23[1])), false, false);
    float r = asf(pq[0]) + asf(pq[1]);
    r += dpp<ROR4>(r); r += dpp<ROR8>(r);
    if ((lane & 12) == 0) *(unsigned short*)(p.out + (16 * ci + chn) * 2) = (unsigned short)cvtpk(fmaf(r, gt, xo), 0.f);
    if (ci == 0 && lane == 0) __hip_atomic_store((GAS unsigned*)(p.ws + WS_EPOCH), tag, RLX_AGENT);
    STAMP(9);
  }
  if (blk == 2 && wave == 0) { while ((int)(NOW() - t00) < 3000) __builtin_amdgcn_s_sleep(1); }
}
}

extern "C" void kernel_launch(void* const* d_in, const int* in_sizes, int n_in, void* d_out, int out_size, void* d_ws, size_t ws_size,
                              hipStream_t stream) {
  Args a;
  a.x = (const unsigned char*)d_in[0]; a.k = (const unsigned char*)d_in[1]; a.v = (const unsigned char*)d_in[2];
  a.wq = (const unsigned char*)d_in[3]; a.wo = (const unsigned char*)d_in[4];
  a.w1 = (const unsigned char*)d_in[6]; a.b1 = (const unsigned char*)d_in[7]; a.w2 = (const unsigned char*)d_in[8]; a.b2 = (const unsigned char*)d_in[9];
  a.out = (unsigned char*)d_out; a.ws = (unsigned char*)d_ws;
  hipLaunchKernelGGL(fused_kernel, dim3(256), dim3(NTHR), 0, stream, a);
}
```

```cpp
#include <hip/hip_runtime.h>
#include <hip/hip_bf16.h>
#include <stdint.h>

typedef unsigned v4u __attribute__((ext_vector_type(4)));
typedef unsigned v2u __attribute__((ext_vector_type(2)));
typedef float f32x4 __attribute__((ext_vector_type(4)));
typedef float f32x2 __attribute__((ext_vector_type(2)));
typedef short bf16x8 __attribute__((ext_vector_type(8)));
#define LAS __attribute__((address_space(3)))
#define GAS __attribute__((address_space(1)))
#define RLX_AGENT __ATOMIC_RELAXED, __HIP_MEMORY_SCOPE_AGENT
#define RLX_WG __ATOMIC_RELAXED, __HIP_MEMORY_SCOPE_WORKGROUP

#define T_FIRST 105
#define TAU4 118
#define T_STAG 4
#define T_MARGIN 10
#define T_VIS 120
#define SC_MIN 230
#define SC_MAX 307
#define WINDOW 8
#define Q_AT 330
#define Y_LEAD 100
#define QKV 2
#define ML_SLEEP 24
#define WO_AT 10
#define O_DELAY 15
#define S_DELAY 11
#define WO_EARLY 2
#define A0 95
#define AW 4
#define B_GAP 30
#define B_MAX 35
#define CODE_PF 19520
#define CUM(n) ((n) * TAU4)

namespace {
constexpr int DM = 576, SK = 8192, WIN = 2048;
constexpr int NTHR = 768;
constexpr int AUX_NT = 2, AUX_SC1 = 16, RSRC_W3 = 0x00020000;
constexpr int OOB = 0x7ffffff0;
constexpr unsigned SPIN_CAP = 1u << 17;
constexpr int WS_EPOCH = 0;
constexpr int WS_TB = 4096;
constexpr int WS_QP = WS_TB + 32768;
constexpr int OP_SRC = 1152;
constexpr int WS_OP = WS_QP + 16 * 4 * 3 * 1024;
constexpr int WS_SP = WS_OP + 64 * 4 * OP_SRC;
constexpr int WS_YP = WS_SP + 64 * 9216;
constexpr int WS_END = WS_YP + 256 * 256;
constexpr int L_K = 0;
constexpr int L_O = 131072;
constexpr int L_QP = L_O + 8192;
constexpr int L_Q = L_QP + 3072;
constexpr int L_ML = L_Q + 512;
constexpr int L_MIX = L_ML + 128;
constexpr int L_X = L_MIX + 512;
constexpr int L_XN = L_X + 4608;
constexpr int L_ST = L_XN + 4608;
constexpr int L_FL = L_ST + 32;
constexpr int L_BYTES = L_FL + 64;
enum { F_QP = 0, F_Q = 1, F_ML = 2, F_VI = 3, F_O = 4, F_GATH = 5, F_MIX = 6, F_LN = 7, F_END = 8, F_ORG = 9, F_TAG = 10 };

struct Args {
  const unsigned char* x; const unsigned char* k; const unsigned char* v; const unsigned char* wq; const unsigned char* wo;
  const unsigned char* w1; const unsigned char* b1; const unsigned char* w2; const unsigned char* b2;
  unsigned char* out; unsigned char* ws;
};

__device__ __forceinline__ __amdgpu_buffer_rsrc_t mkrs(const void* p, int bytes) { return __builtin_amdgcn_make_buffer_rsrc((void*)p, 0, bytes, RSRC_W3); }
template <int AUX> __device__ __forceinline__ v4u ld16(__amdgpu_buffer_rsrc_t rs, int voff, int soff = 0) { return __builtin_amdgcn_raw_buffer_load_b128(rs, voff, soff, AUX); }
template <int AUX> __device__ __forceinline__ unsigned ld4(__amdgpu_buffer_rsrc_t rs, int voff) { return __builtin_amdgcn_raw_buffer_load_b32(rs, voff, 0, AUX); }
__device__ __forceinline__ unsigned ld2(__amdgpu_buffer_rsrc_t rs, int voff) { return __builtin_amdgcn_raw_buffer_load_b16(rs, voff, 0, 0); }
__device__ __forceinline__ void st16_sc1(__amdgpu_buffer_rsrc_t rs, int voff, v4u v) { __builtin_amdgcn_raw_buffer_store_b128(v, rs, voff, 0, AUX_SC1); }
__device__ __forceinline__ float asf(unsigned u) { return __builtin_bit_cast(float, u); }
__device__ __forceinline__ unsigned asu(float f) { return __builtin_bit_cast(unsigned, f); }
__device__ __forceinline__ float bflo(unsigned u) { return asf(u << 16); }
__device__ __forceinline__ float bfhi(unsigned u) { return asf(u & 0xffff0000u); }
__device__ __forceinline__ float rdlane(float x, int l) { return asf((unsigned)__builtin_amdgcn_readlane((int)asu(x), l)); }
__device__ __forceinline__ unsigned cvtpk(float lo, float hi) {
  typedef __bf16 b2 __attribute__((ext_vector_type(2)));
  b2 r = {(__bf16)lo, (__bf16)hi};
  return __builtin_bit_cast(unsigned, r);
}
template <int CTRL> __device__ __forceinline__ float dpp(float x) {
  return __builtin_bit_cast(float, __builtin_amdgcn_mov_dpp(__builtin_bit_cast(int, x), CTRL, 0xf, 0xf, true));
}
template <int CTRL> __device__ __forceinline__ unsigned dppu(unsigned x) { return (unsigned)__builtin_amdgcn_mov_dpp((int)x, CTRL, 0xf, 0xf, true); }
constexpr int XOR1 = 0xB1, XOR2 = 0x4E, XOR7 = 0x141, ROR8 = 0x128, ROR4 = 0x124, BCAST = 0x150;
__device__ __forceinline__ float xrow_sum(float x) {
  auto s = __builtin_amdgcn_permlane16_swap(asu(x), asu(x), false, false);
  x = asf(s[0]) + asf(s[1]);
  auto t = __builtin_amdgcn_permlane32_swap(asu(x), asu(x), false, false);
  return asf(t[0]) + asf(t[1]);
}
__device__ __forceinline__ float xrow_max(float x) {
  auto s = __builtin_amdgcn_permlane16_swap(asu(x), asu(x), false, false);
  x = fmaxf(asf(s[0]), asf(s[1]));
  auto t = __builtin_amdgcn_permlane32_swap(asu(x), asu(x), false, false);
  return fmaxf(asf(t[0]), asf(t[1]));
}
__device__ __forceinline__ f32x4 mfma(v4u a, v4u b, f32x4 c) {
  return __builtin_amdgcn_mfma_f32_16x16x32_bf16(__builtin_bit_cast(bf16x8, a), __builtin_bit_cast(bf16x8, b), c, 0, 0, 0);
}
__device__ __forceinline__ v4u colfrag(const v4u* r, int j) {
  const unsigned sel = (j & 1) ? 0x07060302u : 0x05040100u;
  v4u f;
  f[0] = __builtin_amdgcn_perm(r[1][j >> 1], r[0][j >> 1], sel);
  f[1] = __builtin_amdgcn_perm(r[3][j >> 1], r[2][j >> 1], sel);
  f[2] = __builtin_amdgcn_perm(r[5][j >> 1], r[4][j >> 1], sel);
  f[3] = __builtin_amdgcn_perm(r[7][j >> 1], r[6][j >> 1], sel);
  return f;
}
#define CBAR() asm volatile("" ::: "memory")
#define LDS_DRAIN() asm volatile("s_waitcnt lgkmcnt(0)" ::: "memory")
#define SCHED() __builtin_amdgcn_sched_barrier(0)
#define VM_WAIT(n) do { SCHED(); asm volatile("s_waitcnt vmcnt(%0)" :: "n"(n) : "memory"); SCHED(); } while (0)
#define NOW() ((unsigned)__builtin_amdgcn_s_memrealtime())

template <int SLEEP> __device__ __forceinline__ void lds_wait_ge(volatile LAS unsigned* w, unsigned want) {
#pragma unroll 1
  for (unsigned spins = 0; spins < (SPIN_CAP << 4); ++spins) {
    if (__builtin_amdgcn_readfirstlane(*w) >= want) break;
    if (SLEEP) __builtin_amdgcn_s_sleep(SLEEP);
  }
  CBAR();
}
__device__ __forceinline__ void lds_arrive(LAS unsigned char* L, int word, int lane) {
  LDS_DRAIN();
  if (lane == 0) __hip_atomic_fetch_add((LAS unsigned*)(L + L_FL + 4 * word), 1u, RLX_WG);
}
__device__ __forceinline__ unsigned next_tag(unsigned e) {
  unsigned h = e * 0x9E3779B1u + 0x7F4A7C15u;
  h = (h ^ (h >> 15)) | 0x01010101u;
  return ((e & 0x7f7f7f7fu) + (h & 0x7f7f7f7fu)) ^ ((e ^ h) & 0x80808080u);
}
__device__ __forceinline__ v4u pack4(f32x4 x, unsigned tag) {
  return v4u{__builtin_amdgcn_perm(asu(x[0]), tag, 0x07060500u), __builtin_amdgcn_perm(asu(x[1]), tag, 0x07060501u),
             __builtin_amdgcn_perm(asu(x[2]), tag, 0x07060502u), __builtin_amdgcn_perm(asu(x[3]), tag, 0x07060503u)};
}
__device__ __forceinline__ float q24(float x) { return asf(asu(x) & 0xffffff00u); }
__device__ __forceinline__ f32x4 unpack4(v4u v) { return f32x4{asf(v[0] & 0xffffff00u), asf(v[1] & 0xffffff00u), asf(v[2] & 0xffffff00u), asf(v[3] & 0xffffff00u)}; }
__device__ __forceinline__ bool tagged(v4u v, unsigned tag) {
  const unsigned lo = __builtin_amdgcn_perm(v[1], v[0], 0x0c0c0400u), hi = __builtin_amdgcn_perm(v[3], v[2], 0x04000c0cu);
  return (lo | hi) == tag;
}
template <int N, int STRIDE, int SLEEP> __device__ __forceinline__ f32x4 gather_sum(__amdgpu_buffer_rsrc_t rws, int off, unsigned tag) {
  v4u t[N];
  bool good;
  unsigned spins = 0;
#pragma unroll 1
  do {
    if (SLEEP && spins) __builtin_amdgcn_s_sleep(SLEEP);
    CBAR();
#pragma unroll
    for (int i = 0; i < N; ++i) t[i] = ld16<AUX_SC1>(rws, off, i * STRIDE);
    good = true;
#pragma unroll
    for (int i = 0; i < N; ++i) good &= tagged(t[i], tag);
  } while (!good && ++spins < SPIN_CAP);
  f32x4 s = unpack4(t[0]);
  if (N == 4) s = (s + unpack4(t[1])) + (unpack4(t[2]) + unpack4(t[3]));
  else {
#pragma unroll
    for (int i = 1; i < N; ++i) s += unpack4(t[i]);
  }
  if (!good) s[0] = __builtin_nanf("");
  return s;
}
__device__ __forceinline__ f32x4 project(v4u af, const v4u (&wf)[8], int lane) {
  f32x4 d[8];
#pragma unroll
  for (int k = 0; k < 4; ++k) { d[k] = mfma(af, wf[k], f32x4{0.f, 0.f, 0.f, 0.f}); SCHED(); d[4 + k] = mfma(af, wf[4 + k], f32x4{0.f, 0.f, 0.f, 0.f}); SCHED(); }
  const bool up = (lane & 8) != 0;
  f32x4 u;
#pragma unroll
  for (int k = 0; k < 4; ++k) {
    asm volatile("" : "+v"(d[k]), "+v"(d[4 + k]));
    const float keep = up ? d[4 + k][1] : d[k][0], send = up ? d[k][1] : d[4 + k][0];
    SCHED();
    u[k] = keep + dpp<ROR8>(send);
    SCHED();
  }
  return u;
}

__global__ void __launch_bounds__(NTHR, 3) fused_kernel(Args p) {
  const int blk = blockIdx.x, idx = blk >> 3;
  const unsigned long long tbase = ((const __attribute__((address_space(4))) unsigned long long*)__builtin_amdgcn_kernarg_segment_ptr())
      [(((idx & 16) ? 0x2121AA2198217621ull : 0x5021442133213321ull) >> (4 * (idx & 15))) & 15u];
  SCHED();
  const unsigned pf = ld4<0>(mkrs((const void*)(__builtin_amdgcn_s_getpc() & ~255ull), CODE_PF), ((int)threadIdx.x - 384) * 64);
  SCHED();
  __shared__ __attribute__((aligned(16))) unsigned char smem[L_BYTES];
  LAS unsigned char* L = (LAS unsigned char*)smem;
  volatile LAS unsigned* fl = (volatile LAS unsigned*)(L + L_FL);
  const int tid = threadIdx.x, lane = tid & 63, wave = __builtin_amdgcn_readfirstlane(tid >> 6);
  const int c = lane & 15, g = lane >> 4;
  const int kvh = 2 * (blk & 7) + (idx >> 4), sub = idx & 15, b = sub >> 2, split = sub & 3, pair = b * 16 + kvh;
  unsigned tch, tstart;
  unsigned long long t64;
  {
    const unsigned kind = (unsigned)(((idx & 16) ? 0x9911801100110011ull : 0x0011A21164114011ull) >> (4 * (idx & 15)));
    unsigned off = (kind & 1u) * ((unsigned)(pair * SK + (SK - WIN)) * 256u) + ((kind >> 3) & 1u) * 524286u;
    off += ((kind & 6u) == 2u ? (blk & 7) * 512u * (DM * 2u) : 0u) + ((kind & 6u) == 4u ? (2u << 20) << (idx >> 2) : 0u) + ((kind & 6u) == 6u ? DM * 8192u - 2u : 0u);
    int tvo = (lane == 0 && wave < 4) ? 0 : OOB;
    asm volatile("" : "+s"(off), "+v"(tvo));
    SCHED();
    const __amdgpu_buffer_rsrc_t rt = mkrs((const void*)(tbase + off), 2);
    asm volatile("" :: "s"(rt));
    t64 = __builtin_amdgcn_s_memrealtime();
    SCHED();
    tch = __builtin_amdgcn_raw_buffer_load_b16(rt, tvo, 0, AUX_SC1);
  }
  SCHED();
  const __amdgpu_buffer_rsrc_t rws = mkrs(p.ws, WS_END);
#define INIT_BARRIER() do { SCHED(); asm volatile("" : "+s"(t64)); tstart = (unsigned)t64; if (tid < 16) fl[tid] = tid == F_END ? tstart + T_FIRST + CUM(31) / 4 : (tid == F_ORG ? tstart : 0u); \
    LDS_DRAIN(); __builtin_amdgcn_s_barrier(); CBAR(); tstart = __builtin_amdgcn_readfirstlane(fl[F_ORG]); SCHED(); } while (0)
  const int ngrp = b == 3 ? 3 : 2;

  if (wave >= 4) {
    const int a = wave - 4;
    const size_t kvoff = ((size_t)pair * SK + (SK - WIN) + split * 512 + a * 64) * 256;
    const __amdgpu_buffer_rsrc_t rk = mkrs(p.k + kvoff, 16384), rv = mkrs(p.v + kvoff, 16384);
    const unsigned ldsk = (unsigned)(uintptr_t)(L + L_K) + a * 16384;
    const int hr = ((idx + 2 * a) & 15) << 10;
    int kv[4];
#pragma unroll
    for (int r = 0; r < 4; ++r) kv[r] = g * 256 + ((c ^ (4 * g + r)) << 4);
    __builtin_amdgcn_s_setprio(2);
    v4u wr[8];
    const __amdgpu_buffer_rsrc_t ro = mkrs(p.wo, a < ngrp ? 4096 * DM * 2 : 0);
    const int wo_off = (kvh * 256 + 64 * split + 8 * (4 * (c >> 3) + g)) * (DM * 2) + (2 * b + a) * 128 + (c & 7) * 16;
#pragma unroll
    for (int jj = 0; jj < WO_EARLY; ++jj) wr[jj] = ld16<AUX_NT>(ro, wo_off, jj * (DM * 2));
    INIT_BARRIER();
    unsigned tref = tstart + T_FIRST;
    {
      const int slow = (int)(NOW() + B_GAP - tref);
      tref += slow < 0 ? 0 : (slow > B_MAX ? B_MAX : slow);
    }
    int scale = 256;
    const int stag = (a - 7) * T_STAG;
#define RESPREAD(n) do { const unsigned at_ = tref + (CUM(n) * scale >> 10); \
      int s_ = ((int)(__builtin_amdgcn_readfirstlane(fl[F_END]) - at_) * ((1 << 22) / (CUM(31) - CUM(n)))) >> 12; \
      scale = s_ < SC_MIN ? SC_MIN : (s_ > SC_MAX ? SC_MAX : s_); tref = at_ - (CUM(n) * scale >> 10); } while (0)
#define SLOT(n) do { SCHED(); if ((n) == 4 || (n) == 8 || (n) == 12 || (n) == 16 || (n) == 27) RESPREAD((n) - 1); \
      while ((int)(NOW() - tref - stag) < (CUM(n) * scale >> 10)) { if ((n) < 31) __builtin_amdgcn_s_sleep(1); } SCHED(); } while (0)
#define V_AT(i, n) do { SLOT(n); vr[i] = ld16<AUX_NT>(rv, lane * 16, ((i) << 10) ^ hr); VM_WAIT(WINDOW); } while (0)
#define V_PIECE(i) V_AT(i, 16 + (i))
#pragma unroll
    for (int i = 0; i < 16; ++i) {
      SLOT(i);
      unsigned keep;
      asm volatile("s_mov_b32 %0, m0\n\ts_mov_b32 m0, %1\n\ts_nop 0\n\tbuffer_load_dwordx4 %2, %3, %4 offen nt lds\n\ts_mov_b32 m0, %0"
                   : "=&s"(keep) : "s"(ldsk + i * 1024), "v"(kv[i & 3]), "s"(rk), "s"((i << 10) ^ hr) : "memory");
      VM_WAIT(WINDOW);
    }
    v4u vr[16];
#pragma unroll
    for (int i = 0; i < QKV; ++i) V_PIECE(i);
    V_AT(QKV, 15 + QKV);
    asm volatile("" :: "v"(pf));
    asm volatile("" :: "v"(tch));
    lds_wait_ge<1>(fl + F_Q, 1u);
    v4u qf[4];
#pragma unroll
    for (int kk = 0; kk < 4; ++kk) qf[kk] = *(const LAS v4u*)(L + L_Q + ((c >> 2) & 1) * 256 + kk * 64 + g * 16);
    VM_WAIT(QKV + 1);
    f32x4 S[4];
    {
      const LAS unsigned char* kb = L + L_K + a * 16384 + (c & 3) * 1024 + (c >> 2) * 256;
#pragma unroll
      for (int t = 0; t < 4; ++t) S[t] = f32x4{0.f, 0.f, 0.f, 0.f};
#pragma unroll
      for (int kk = 0; kk < 4; ++kk) {
        v4u kf[4];
#pragma unroll
        for (int t = 0; t < 4; ++t) kf[t] = *(const LAS v4u*)(kb + t * 4096 + (((4 * kk) ^ g ^ c) << 4));
#pragma unroll
        for (int t = 0; t < 4; ++t) S[t] = mfma(kf[t], qf[kk], S[t]);
      }
    }
    asm volatile("" : "+v"(S[0]), "+v"(S[1]), "+v"(S[2]), "+v"(S[3]));
    V_PIECE(QKV + 1);
    constexpr float SC = 0.088388347648318440f * 1.4426950408889634f;
    float mx = -INFINITY;
#pragma unroll
    for (int t = 0; t < 4; ++t) mx = fmaxf(mx, fmaxf(fmaxf(S[t][0], S[t][1]), fmaxf(S[t][2], S[t][3])));
    mx = xrow_max(mx) * SC;
    float ls = 0.f;
    v4u pr[2];
#pragma unroll
    for (int t = 0; t < 4; ++t) {
      float e[4];
#pragma unroll
      for (int r = 0; r < 4; ++r) e[r] = __builtin_amdgcn_exp2f(fmaf(S[t][r], SC, -mx));
      ls += (e[0] + e[1]) + (e[2] + e[3]);
      pr[t >> 1][2 * (t & 1)] = cvtpk(e[0], e[1]);
      pr[t >> 1][2 * (t & 1) + 1] = cvtpk(e[2], e[3]);
    }
    ls = xrow_sum(ls);
    if ((lane & 0x3b) == 0) *(LAS f32x2*)(L + L_ML + (lane >> 2) * 64 + a * 8) = f32x2{mx, ls};
    lds_arrive(L, F_ML, lane);
    asm volatile("" : "+v"(pr[0]), "+v"(pr[1]));
    V_PIECE(QKV + 2);
    unsigned oaddr = L_O + a * 1024 + (g & 1) * 512 + c * 32 + (g >> 1) * 16;
    asm volatile("" : "+v"(oaddr));
#pragma unroll
    for (int i = QKV + 3; i < 13; ++i) V_PIECE(i);
    f32x4 acc[8];
#pragma unroll
    for (int j = 0; j < 8; ++j) { acc[j] = mfma(pr[0], colfrag(vr, j), f32x4{0.f, 0.f, 0.f, 0.f}); asm volatile("" : "+v"(acc[j])); }
    v4u vf[8];
#define V_PAIR(d) do { _Pragma("unroll") for (int j = 0; j < 8; ++j) { \
      unsigned t_ = __builtin_amdgcn_perm(vr[9 + 2 * (d)][j >> 1], vr[8 + 2 * (d)][j >> 1], (j & 1) ? 0x07060302u : 0x05040100u); \
      asm volatile("" : "+v"(t_)); vf[j][d] = t_; } SCHED(); } while (0)
    V_PIECE(13);
    V_PIECE(14);
    V_PAIR(0);
    V_PIECE(15);
    if (lane == 0) __hip_atomic_fetch_add((LAS unsigned*)(L + L_FL + 4 * F_VI), 1u, RLX_WG);
    SCHED();
    V_PAIR(1);
    V_PAIR(2);
    auto finish = [&]() {
      V_PAIR(3);
#pragma unroll
      for (int j = 0; j < 8; ++j) acc[j] = mfma(pr[1], vf[j], acc[j]);
      asm volatile("" : "+v"(acc[0]), "+v"(acc[1]), "+v"(acc[2]), "+v"(acc[3]), "+v"(acc[4]), "+v"(acc[5]), "+v"(acc[6]), "+v"(acc[7]));
      const bool hi = (lane & 32) != 0;
      *(LAS f32x4*)(L + oaddr) = f32x4{hi ? acc[4][0] : acc[0][0], hi ? acc[5][0] : acc[1][0], hi ? acc[6][0] : acc[2][0], hi ? acc[7][0] : acc[3][0]};
      CBAR();
      if (lane == 0) __hip_atomic_fetch_add((LAS unsigned*)(L + L_FL + 4 * F_O), 1u, RLX_WG);
      SCHED();
    };
    if (a >= ngrp) {
      finish();
      if (a == 3) {
        const unsigned wm = ld4<AUX_SC1>(rws, lane < 8 * ngrp ? WS_SP + (kvh * 4 + split) * 9216 + 2 * b * 1024 + lane * 128 : OOB);
        asm volatile("" :: "v"(wm));
      }
      return;
    }
    {
      lds_wait_ge<1>(fl + F_VI, 8u);
      __builtin_amdgcn_s_sleep(WO_AT);
#pragma unroll
      for (int jj = WO_EARLY; jj < 8; ++jj) wr[jj] = ld16<AUX_NT>(ro, wo_off, jj * (DM * 2));
      SCHED();
      finish();
      const unsigned tag = __builtin_amdgcn_readfirstlane(fl[F_TAG]);
      const bool ph = (c & 2) != 0;
      int oo = WS_OP + ((c >> 2) * 16 + kvh) * 4 * OP_SRC + (64 * split + 8 * (4 * (c & 1) + g) + (c & 2) * 2) * 4;
      int so = WS_SP + (kvh * 4 + split) * 9216 + (2 * b + a) * 1024 + g * 256 + (c & 7) * 32 + (c >> 3) * 16;
      asm volatile("" : "+v"(so), "+v"(oo));
      lds_wait_ge<0>(fl + F_O, 8u);
      __builtin_amdgcn_s_sleep(O_DELAY);
      CBAR();
      v4u t[4];
#pragma unroll
      for (int i = 0; i < 4; ++i) t[i] = ld16<AUX_SC1>(rws, oo, i * OP_SRC);
      SCHED();
      v4u wf[8];
#pragma unroll
      for (int j = 0; j < 8; ++j) { wf[j] = colfrag(wr, j); asm volatile("" : "+v"(wf[j])); }
      SCHED();
      f32x4 x = (unpack4(t[0]) + unpack4(t[1])) + (unpack4(t[2]) + unpack4(t[3]));
      if (!(tagged(t[0], tag) & tagged(t[1], tag) & tagged(t[2], tag) & tagged(t[3], tag))) x = gather_sum<4, OP_SRC, 0>(rws, oo, tag);
      const unsigned p0 = cvtpk(x[0], x[1]), p1 = cvtpk(x[2], x[3]);
      const unsigned q0 = dppu<XOR2>(p0), q1 = dppu<XOR2>(p1);
      const v4u af = {ph ? q0 : p0, ph ? q1 : p1, ph ? p0 : q0, ph ? p1 : q1};
      st16_sc1(rws, so, pack4(project(af, wf, lane), tag));
      if (lane == 0) __hip_atomic_fetch_add((LAS unsigned*)(L + L_FL + 4 * F_GATH), 1u, RLX_WG);
    }
    return;
  }

  __builtin_amdgcn_s_setprio(3);
  const bool qrole = (sub < 12) && (wave < 3);
  const unsigned epoch = ld4<AUX_SC1>(rws, (wave == 3 && lane == 0) ? WS_EPOCH : OOB);
  const __amdgpu_buffer_rsrc_t rx = mkrs(p.x + wave * (DM * 2), DM * 2);
  v4u xr = ld16<0>(rx, lane * 16);
  unsigned xt = ld2(rx, 1024 + lane * 2);
  const int mt = tid & 127;
  v4u m1, mb, m2;
  unsigned m1t, mbt, m2t, tag, warm, b2v = 0u;
  auto more_loads = [&]() {
    SCHED();
    const int fo = (blk & 3) * 2304;
    const __amdgpu_buffer_rsrc_t r1 = mkrs(p.w1 + fo, 2304), rb = mkrs(p.b1 + fo, 2304), r2 = mkrs(p.w2 + fo, 2304);
    m1t = ld2(r1, 2048 + mt * 2); mbt = ld2(rb, 2048 + mt * 2); m2t = ld2(r2, 2048 + mt * 2);
    m1 = ld16<0>(r1, mt * 16); mb = ld16<0>(rb, mt * 16); m2 = ld16<0>(r2, mt * 16);
    b2v = ld2(mkrs(p.b2, 2), wave == 3 ? 0 : OOB);
    warm = ld4<AUX_SC1>(rws, (wave == 3 && lane < 11) ? (lane < 9 ? WS_OP + (pair * 4 + split) * OP_SRC : WS_YP + blk * 256 - 9 * 128) + lane * 128 : OOB);
    SCHED();
  };
  auto layernorm = [&]() {
    INIT_BARRIER();
    asm volatile("" : "+v"(xr), "+v"(xt));
    tag = next_tag(__builtin_amdgcn_readfirstlane(epoch));
    if (wave == 3 && idx == 0) {
      const int slow = (int)(NOW() - tstart) - A0;
      st16_sc1(rws, lane < 32 ? WS_TB + blk * 4096 + lane * 128 : OOB, pack4(f32x4{asf((tstart + (slow > 0 ? slow * AW >> 3 : 0)) << 8), 0.f, 0.f, 0.f}, tag));
    }
    f32x2 xv[4];
#pragma unroll
    for (int i = 0; i < 4; ++i) xv[i] = f32x2{bflo(xr[i]), bfhi(xr[i])};
    const float x8 = bflo(xt);
    const f32x2 st = (xv[0] + xv[1]) + (xv[2] + xv[3]);
    float s = st[0] + st[1] + x8;
    s += dpp<XOR1>(s); s += dpp<XOR2>(s); s += dpp<ROR4>(s); s += dpp<ROR8>(s); s = xrow_sum(s);
    const float mean = s * (1.f / DM);
#pragma unroll
    for (int i = 0; i < 4; ++i) xv[i] -= mean;
    const float e8 = x8 - mean;
    const f32x2 qt = (xv[0] * xv[0] + xv[1] * xv[1]) + (xv[2] * xv[2] + xv[3] * xv[3]);
    float q = fmaf(e8, e8, qt[0] + qt[1]);
    q += dpp<XOR1>(q); q += dpp<XOR2>(q); q += dpp<ROR4>(q); q += dpp<ROR8>(q); q = xrow_sum(q);
    const float var = q * (1.f / DM);
    const float rstd = __builtin_amdgcn_rsqf(var + 1e-5f);
    const float hs = rstd * __builtin_amdgcn_rsqf(var * rstd * rstd + 1e-5f);
    LAS unsigned char* row = L + wave * (DM * 2);
    *(LAS v4u*)(row + L_XN + lane * 16) = v4u{cvtpk(xv[0][0] * rstd, xv[0][1] * rstd), cvtpk(xv[1][0] * rstd, xv[1][1] * rstd),
                                              cvtpk(xv[2][0] * rstd, xv[2][1] * rstd), cvtpk(xv[3][0] * rstd, xv[3][1] * rstd)};
    *(LAS unsigned short*)(row + L_XN + 1024 + lane * 2) = (unsigned short)cvtpk(e8 * rstd, 0.f);
    *(LAS v4u*)(row + L_X + lane * 16) = xr;
    *(LAS unsigned short*)(row + L_X + 1024 + lane * 2) = (unsigned short)xt;
    if (lane == 0) { *(LAS f32x2*)(L + L_ST + wave * 8) = f32x2{mean, hs}; if (wave == 3) fl[F_TAG] = tag; }
    lds_arrive(L, F_LN, lane);
    asm volatile("" :: "v"(pf));
    asm volatile("" :: "v"(tch));
    lds_wait_ge<0>(fl + F_LN, 4u);
    tag = __builtin_amdgcn_readfirstlane(fl[F_TAG]);
  };
  if (__builtin_expect(qrole, 1)) {
    const int qi = (idx >> 4) * 12 + sub, ks = qi >> 3, G = 8 * (qi & 7) + ((blk + qi) & 7);
    const int R0 = 192 * ks + 64 * wave;
    v4u wr[8];
    const __amdgpu_buffer_rsrc_t rq = mkrs(p.wq, DM * 4096 * 2);
    const int off = (R0 + 8 * (4 * (c >> 3) + g)) * 8192 + G * 128 + (c & 7) * 16;
#pragma unroll
    for (int jj = 0; jj < 8; ++jj) wr[jj] = ld16<AUX_NT>(rq, off, jj * 8192);
    more_loads();
    layernorm();
    const v4u af = *(const LAS v4u*)(L + L_XN + (c >> 2) * (DM * 2) + (R0 + 8 * (4 * (c & 1) + g)) * 2);
    v4u wf[8];
#pragma unroll
    for (int j = 0; j < 8; ++j) wf[j] = colfrag(wr, j);
    *(LAS f32x4*)(L + L_QP + wave * 1024 + g * 256 + (c & 7) * 32 + (c >> 3) * 16) = project(af, wf, lane);
    lds_arrive(L, F_QP, lane);
    if (wave == 2) {
      lds_wait_ge<0>(fl + F_QP, 3u);
      const LAS f32x4* sq = (const LAS f32x4*)(L + L_QP + lane * 16);
      st16_sc1(rws, WS_QP + (((G >> 2) * 4 + g) * 3 + ks) * 1024 + (G & 3) * 256 + c * 16, pack4(sq[0] + sq[64] + sq[128], tag));
    }
  } else {
    more_loads();
    layernorm();
  }
  __builtin_amdgcn_s_setprio(0);
  {
    const int ch0 = 36 * (blk >> 2) + 18 * (wave >> 1);
    const f32x2 stt = *(const LAS f32x2*)(L + L_ST + (blk >> 6) * 8);
    const unsigned xh = *(const LAS unsigned*)(L + L_X + (ch0 + 2 * (c < 9 ? c : 0)) * 2);
    const float h0 = (bflo(xh) - stt[0]) * stt[1], h1 = (bfhi(xh) - stt[0]) * stt[1];
    float w1[9], bb[9], w2[9];
#pragma unroll
    for (int i = 0; i < 4; ++i) {
      w1[2 * i] = bflo(m1[i]); w1[2 * i + 1] = bfhi(m1[i]); bb[2 * i] = bflo(mb[i]); bb[2 * i + 1] = bfhi(mb[i]); w2[2 * i] = bflo(m2[i]); w2[2 * i + 1] = bfhi(m2[i]);
    }
    w1[8] = bflo(m1t); bb[8] = bflo(mbt); w2[8] = bflo(m2t);
    constexpr float C1 = -2.0f * 0.7978845608028654f * 1.4426950408889634f, C2 = C1 * 0.044715f;
    float ykeep = 0.f;
    bool have_q = wave != 1;
    asm volatile("" :: "v"(warm));
#pragma unroll 1
    for (int it = 0; it < 9; ++it) {
      if ((wave == 3 && it == 0) || (wave == 2 && it == 2)) {
        CBAR();
        const int guess = (int)(NOW() - tstart) - T_VIS;
        const v4u tb = ld16<AUX_SC1>(rws, lane < 8 ? WS_TB + lane * 4096 + idx * 128 : OOB);
        float d = tagged(tb, tag) ? (float)((int)((tb[0] & 0xffffff00u) - (tstart << 8)) >> 8) : (float)(guess > 0 ? guess : 0);
        d += dpp<XOR1>(d); d += dpp<XOR2>(d); d += dpp<XOR7>(d);
        if (lane == 0) fl[F_END] = tstart + (int)(d * 0.125f) + T_MARGIN + T_FIRST + CUM(31) / 4;
      }
      if (!have_q && (it == 8 || (int)(NOW() + (T_FIRST + CUM(31) / 4 - Q_AT) - __builtin_amdgcn_readfirstlane(fl[F_END])) >= 0)) {
        have_q = true;
        __builtin_amdgcn_s_setprio(3);
        const f32x4 qs = gather_sum<3, 1024, 8>(rws, WS_QP + (kvh * 4 + b) * 3 * 1024 + lane * 16, tag);
        *(LAS v2u*)(L + L_Q + lane * 8) = v2u{cvtpk(qs[0], qs[1]), cvtpk(qs[2], qs[3])};
        lds_arrive(L, F_Q, lane);
        __builtin_amdgcn_s_setprio(0);
      }
      const f32x2 h = {rdlane(h0, it), rdlane(h1, it)};
      f32x2 acc = {0.f, 0.f};
#pragma unroll
      for (int i = 0; i < 9; ++i) {
        const f32x2 u = h * w1[i] + bb[i];
        const f32x2 z = u * (u * u * C2 + C1);
        const f32x2 d = f32x2{__builtin_amdgcn_exp2f(z[0]), __builtin_amdgcn_exp2f(z[1])} + 1.0f;
        const f32x2 r = {__builtin_amdgcn_rcpf(d[0]), __builtin_amdgcn_rcpf(d[1])};
        acc = (u * w2[i]) * r + acc;
      }
      const auto sw = __builtin_amdgcn_permlane32_swap(asu(acc[0]), asu(acc[1]), false, false);
      float y = asf(sw[0]) + asf(sw[1]);
      y += dpp<XOR1>(y); y += dpp<XOR2>(y); y += dpp<ROR4>(y); y += dpp<ROR8>(y);
      ykeep = (c == it) ? y : ykeep;
    }
    __builtin_amdgcn_s_setprio(3);
    const auto sy = __builtin_amdgcn_permlane16_swap(asu(ykeep), asu(ykeep), false, false);
    ykeep = asf(sy[0]) + asf(sy[1]);
    if (c < 9 && (g & 1) == 0) *(LAS float*)(L + L_MIX + (wave & 1) * 256 + (18 * (wave >> 1) + 2 * c + (g >> 1)) * 4) = ykeep;
    lds_arrive(L, F_MIX, lane);
    if (wave == 3) {
      lds_wait_ge<2>(fl + F_MIX, 4u);
      const LAS f32x4* sy4 = (const LAS f32x4*)(L + L_MIX + c * 16);
      st16_sc1(rws, lane < 16 ? WS_YP + blk * 256 + lane * 16 : OOB, pack4(sy4[0] + sy4[16], tag));
    }
  }

  if (wave == 0) {
    lds_wait_ge<1>(fl + F_ML, 8u);
    const f32x2 ml = *(const LAS f32x2*)(L + L_ML + (lane >> 5) * 64 + (lane & 7) * 8);
    float M = fmaxf(ml[0], dpp<XOR1>(ml[0])); M = fmaxf(M, dpp<XOR2>(M)); M = fmaxf(M, dpp<XOR7>(M));
    M = q24(M);
    float Ls = __builtin_amdgcn_exp2f(ml[0] - M) * ml[1];
    Ls += dpp<XOR1>(Ls); Ls += dpp<XOR2>(Ls); Ls += dpp<XOR7>(Ls);
    const int ob = WS_OP + pair * 4 * OP_SRC;
    st16_sc1(rws, lane == 0 ? ob + split * OP_SRC + 1024 : OOB, pack4(f32x4{M, Ls, rdlane(M, 32), rdlane(Ls, 32)}, tag));
    __builtin_amdgcn_s_sleep(ML_SLEEP);
    const f32x4 st4 = gather_sum<1, 0, ML_SLEEP>(rws, ob + (lane & 3) * OP_SRC + 1024, tag);
    const float ms = lane < 32 ? st4[0] : st4[2];
    float Mg = fmaxf(ms, dpp<XOR1>(ms)); Mg = fmaxf(Mg, dpp<XOR2>(Mg));
    float Lg = __builtin_amdgcn_exp2f(ms - Mg) * (lane < 32 ? st4[1] : st4[3]);
    Lg += dpp<XOR1>(Lg); Lg += dpp<XOR2>(Lg);
    const float w = __builtin_amdgcn_exp2f(ml[0] - Mg) * __builtin_amdgcn_rcpf(Lg);
    float ww[8] = {dpp<BCAST>(w), dpp<BCAST + 1>(w), dpp<BCAST + 2>(w), dpp<BCAST + 3>(w), dpp<BCAST + 4>(w), dpp<BCAST + 5>(w), dpp<BCAST + 6>(w), dpp<BCAST + 7>(w)};
    int po = ob + split * OP_SRC + lane * 16;
    asm volatile("" : "+v"(po), "+v"(ww[0]), "+v"(ww[1]), "+v"(ww[2]), "+v"(ww[3]), "+v"(ww[4]), "+v"(ww[5]), "+v"(ww[6]), "+v"(ww[7]));
    lds_wait_ge<2>(fl + F_O, 1u);
    lds_wait_ge<0>(fl + F_O, 8u);
    const LAS f32x4* o = (const LAS f32x4*)(L + L_O + lane * 16);
    const f32x4 t = ((o[0] * ww[0] + o[64] * ww[1]) + (o[128] * ww[2] + o[192] * ww[3])) + ((o[256] * ww[4] + o[320] * ww[5]) + (o[384] * ww[6] + o[448] * ww[7]));
    st16_sc1(rws, po, pack4(t, tag));
  } else if (wave == 3 && idx >= 12 && (idx >> 1) != 13) {
    const int ci = (idx - (idx >= 28 ? 14 : 12)) * 8 + (blk & 7), bc = ci / 36, cg = ci % 36;
    const int chn = 4 * (lane & 3) + 2 * (g & 1) + (g >> 1);
    for (unsigned spins = 0; (int)(NOW() + Y_LEAD - __builtin_amdgcn_readfirstlane(fl[F_END])) < 0 && spins < 4096u; ++spins) __builtin_amdgcn_s_sleep(4);
    const int yu = 4 * ci + (lane & 3);
    const f32x4 y4 = gather_sum<1, 0, 16>(rws, WS_YP + (4 * (yu / 9) + (c >> 2)) * 256 + (yu % 9) * 16, tag);
    float yy = (g & 1) ? ((g >> 1) ? y4[3] : y4[2]) : ((g >> 1) ? y4[1] : y4[0]);
    yy += dpp<ROR4>(yy); yy += dpp<ROR8>(yy);
    yy += bflo(b2v);
    float gt = yy * __builtin_amdgcn_rcpf(1.f + __builtin_amdgcn_exp2f(-1.4426950408889634f * yy));
    float xo = bflo(*(const LAS unsigned short*)(L + L_X + (16 * ci + chn) * 2));
    int so = WS_SP + (lane >> 2) * 4 * 9216 + (cg >> 2) * 1024 + bc * 256 + (cg & 3) * 64 + (lane & 3) * 16;
    int oo = lane < 4 ? (16 * ci + 4 * lane) * 2 : OOB;
    asm volatile("" : "+v"(gt), "+v"(xo), "+v"(so), "+v"(oo));
    lds_wait_ge<1>(fl + F_GATH, (unsigned)ngrp);
    if (S_DELAY) __builtin_amdgcn_s_sleep(S_DELAY);
    const f32x4 t = gather_sum<4, 9216, 0>(rws, so, tag);
    const auto p01 = __builtin_amdgcn_permlane32_swap(asu(t[0]), asu(t[1]), false, false), p23 = __builtin_amdgcn_permlane32_swap(asu(t[2]), asu(t[3]), false, false);
    const auto pq = __builtin_amdgcn_permlane16_swap(asu(asf(p01[0]) + asf(p01[1])), asu(asf(p23[0]) + asf(p23[1])), false, false);
    float r = asf(pq[0]) + asf(pq[1]);
    r += dpp<ROR4>(r); r += dpp<ROR8>(r);
    const unsigned ov = asu(fmaf(r, gt, xo));
    const auto o2 = __builtin_amdgcn_permlane32_swap(ov, ov, false, false);
    const unsigned pk = cvtpk(asf(o2[0]), asf(o2[1]));
    const auto o4 = __builtin_amdgcn_permlane16_swap(pk, pk, false, false);
    __builtin_amdgcn_raw_buffer_store_b64(v2u{o4[0], o4[1]}, mkrs(p.out, 4 * DM * 2), oo, 0, AUX_SC1);
    if (ci == 0 && lane == 0) __hip_atomic_store((GAS unsigned*)(p.ws + WS_EPOCH), tag, RLX_AGENT);
  }
}
}

extern "C" void kernel_launch(void* const* d_in, const int* in_sizes, int n_in, void* d_out, int out_size, void* d_ws, size_t ws_size,
                              hipStream_t stream) {
  Args a;
  a.x = (const unsigned char*)d_in[0]; a.k = (const unsigned char*)d_in[1]; a.v = (const unsigned char*)d_in[2];
  a.wq = (const unsigned char*)d_in[3]; a.wo = (const unsigned char*)d_in[4];
  a.w1 = (const unsigned char*)d_in[6]; a.b1 = (const unsigned char*)d_in[7]; a.w2 = (const unsigned char*)d_in[8]; a.b2 = (const unsigned char*)d_in[9];
  a.out = (unsigned char*)d_out; a.ws = (unsigned char*)d_ws;
  hipLaunchKernelGGL(fused_kernel, dim3(256), dim3(NTHR), 0, stream, a);
}
```

```cpp
#include <hip/hip_runtime.h>
#include <hip/hip_bf16.h>
#include <stdint.h>

typedef unsigned v4u __attribute__((ext_vector_type(4)));
typedef unsigned v2u __attribute__((ext_vector_type(2)));
typedef float f32x4 __attribute__((ext_vector_type(4)));
typedef float f32x2 __attribute__((ext_vector_type(2)));
typedef short bf16x8 __attribute__((ext_vector_type(8)));
#define LAS __attribute__((address_space(3)))
#define GAS __attribute__((address_space(1)))
#define RLX_AGENT __ATOMIC_RELAXED, __HIP_MEMORY_SCOPE_AGENT
#define RLX_WG __ATOMIC_RELAXED, __HIP_MEMORY_SCOPE_WORKGROUP

#define T_FIRST 105
#define TAU4 118
#define T_STAG 4
#define T_MARGIN 10
#define T_CATCH 50
#define T_VIS 120
#define SC_MIN 230
#define SC_MAX 307
#define WINDOW 8
#define Q_AT 330
#define Y_LEAD 100
#define QKV 2
#define ML_SLEEP 16
#define ML_RETRY 8
#define WO_AT 10
#define O_DELAY 15
#define S_DELAY 11
#define WO_EARLY 2
#define A0 95
#define AW 4
#define B_GAP 30
#define B_MAX 35
#define CODE_PF 20096
#define CUM(n) ((n) * TAU4)

namespace {
constexpr int DM = 576, SK = 8192, WIN = 2048;
constexpr int NTHR = 768;
constexpr int AUX_NT = 2, AUX_SC1 = 16, RSRC_W3 = 0x00020000;
constexpr int OOB = 0x7ffffff0;
constexpr unsigned SPIN_CAP = 1u << 17;
constexpr int WS_EPOCH = 0;
constexpr int WS_TB = 4096;
constexpr int WS_QP = WS_TB + 32768;
constexpr int OP_SRC = 1152;
constexpr int WS_OP = WS_QP + 16 * 4 * 3 * 1024;
constexpr int WS_SP = WS_OP + 64 * 4 * OP_SRC;
constexpr int WS_YP = WS_SP + 64 * 9216;
constexpr int WS_END = WS_YP + 256 * 256;
constexpr int L_K = 0;
constexpr int L_O = 131072;
constexpr int L_QP = L_O + 8192;
constexpr int L_Q = L_QP + 3072;
constexpr int L_ML = L_Q + 512;
constexpr int L_MIX = L_ML + 128;
constexpr int L_X = L_MIX + 512;
constexpr int L_XN = L_X + 4608;
constexpr int L_FL = L_XN + 4608;
constexpr int L_BYTES = L_FL + 64;
enum { F_QP = 0, F_Q = 1, F_ML = 2, F_VI = 3, F_O = 4, F_GATH = 5, F_MIX = 6, F_LN = 7, F_END = 8, F_ORG = 9, F_TAG = 10 };

struct Args {
  const unsigned char* x; const unsigned char* k; const unsigned char* v; const unsigned char* wq; const unsigned char* wo;
  const unsigned char* w1; const unsigned char* b1; const unsigned char* w2; const unsigned char* b2;
  unsigned char* out; unsigned char* ws;
};

__device__ __forceinline__ __amdgpu_buffer_rsrc_t mkrs(const void* p, int bytes) { return __builtin_amdgcn_make_buffer_rsrc((void*)p, 0, bytes, RSRC_W3); }
template <int AUX> __device__ __forceinline__ v4u ld16(__amdgpu_buffer_rsrc_t rs, int voff, int soff = 0) { return __builtin_amdgcn_raw_buffer_load_b128(rs, voff, soff, AUX); }
template <int AUX> __device__ __forceinline__ unsigned ld4(__amdgpu_buffer_rsrc_t rs, int voff) { return __builtin_amdgcn_raw_buffer_load_b32(rs, voff, 0, AUX); }
__device__ __forceinline__ unsigned ld2(__amdgpu_buffer_rsrc_t rs, int voff) { return __builtin_amdgcn_raw_buffer_load_b16(rs, voff, 0, 0); }
__device__ __forceinline__ void st16_sc1(__amdgpu_buffer_rsrc_t rs, int voff, v4u v) { __builtin_amdgcn_raw_buffer_store_b128(v, rs, voff, 0, AUX_SC1); }
__device__ __forceinline__ float asf(unsigned u) { return __builtin_bit_cast(float, u); }
__device__ __forceinline__ unsigned asu(float f) { return __builtin_bit_cast(unsigned, f); }
__device__ __forceinline__ float bflo(unsigned u) { return asf(u << 16); }
__device__ __forceinline__ float bfhi(unsigned u) { return asf(u & 0xffff0000u); }
__device__ __forceinline__ float rdlane(float x, int l) { return asf((unsigned)__builtin_amdgcn_readlane((int)asu(x), l)); }
__device__ __forceinline__ unsigned cvtpk(float lo, float hi) {
  typedef __bf16 b2 __attribute__((ext_vector_type(2)));
  b2 r = {(__bf16)lo, (__bf16)hi};
  return __builtin_bit_cast(unsigned, r);
}
template <int CTRL> __device__ __forceinline__ float dpp(float x) {
  return __builtin_bit_cast(float, __builtin_amdgcn_mov_dpp(__builtin_bit_cast(int, x), CTRL, 0xf, 0xf, true));
}
template <int CTRL> __device__ __forceinline__ unsigned dppu(unsigned x) { return (unsigned)__builtin_amdgcn_mov_dpp((int)x, CTRL, 0xf, 0xf, true); }
constexpr int XOR1 = 0xB1, XOR2 = 0x4E, XOR7 = 0x141, ROR8 = 0x128, ROR4 = 0x124, BCAST = 0x150;
__device__ __forceinline__ float xrow_sum(float x) {
  auto s = __builtin_amdgcn_permlane16_swap(asu(x), asu(x), false, false);
  x = asf(s[0]) + asf(s[1]);
  auto t = __builtin_amdgcn_permlane32_swap(asu(x), asu(x), false, false);
  return asf(t[0]) + asf(t[1]);
}
__device__ __forceinline__ float xrow_max(float x) {
  auto s = __builtin_amdgcn_permlane16_swap(asu(x), asu(x), false, false);
  x = fmaxf(asf(s[0]), asf(s[1]));
  auto t = __builtin_amdgcn_permlane32_swap(asu(x), asu(x), false, false);
  return fmaxf(asf(t[0]), asf(t[1]));
}
__device__ __forceinline__ f32x4 mfma(v4u a, v4u b, f32x4 c) {
  return __builtin_amdgcn_mfma_f32_16x16x32_bf16(__builtin_bit_cast(bf16x8, a), __builtin_bit_cast(bf16x8, b), c, 0, 0, 0);
}
__device__ __forceinline__ v4u colfrag(const v4u* r, int j) {
  const unsigned sel = (j & 1) ? 0x07060302u : 0x05040100u;
  v4u f;
  f[0] = __builtin_amdgcn_perm(r[1][j >> 1], r[0][j >> 1], sel);
  f[1] = __builtin_amdgcn_perm(r[3][j >> 1], r[2][j >> 1], sel);
  f[2] = __builtin_amdgcn_perm(r[5][j >> 1], r[4][j >> 1], sel);
  f[3] = __builtin_amdgcn_perm(r[7][j >> 1], r[6][j >> 1], sel);
  return f;
}
#define CBAR() asm volatile("" ::: "memory")
#define LDS_DRAIN() asm volatile("s_waitcnt lgkmcnt(0)" ::: "memory")
#define SCHED() __builtin_amdgcn_sched_barrier(0)
#define VM_WAIT(n) do { SCHED(); asm volatile("s_waitcnt vmcnt(%0)" :: "n"(n) : "memory"); SCHED(); } while (0)
#define NOW() ((unsigned)__builtin_amdgcn_s_memrealtime())

template <int SLEEP> __device__ __forceinline__ void lds_wait_ge(volatile LAS unsigned* w, unsigned want) {
#pragma unroll 1
  for (unsigned spins = 0; spins < (SPIN_CAP << 4); ++spins) {
    if (__builtin_amdgcn_readfirstlane(*w) >= want) break;
    if (SLEEP) __builtin_amdgcn_s_sleep(SLEEP);
  }
  CBAR();
}
__device__ __forceinline__ void lds_arrive(LAS unsigned char* L, int word, int lane) {
  LDS_DRAIN();
  if (lane == 0) __hip_atomic_fetch_add((LAS unsigned*)(L + L_FL + 4 * word), 1u, RLX_WG);
}
__device__ __forceinline__ void row_stats(v4u xr, unsigned xt, float& mean, float& rstd) {
  f32x2 xv[4];
#pragma unroll
  for (int i = 0; i < 4; ++i) xv[i] = f32x2{bflo(xr[i]), bfhi(xr[i])};
  const float x8 = bflo(xt);
  const f32x2 st = (xv[0] + xv[1]) + (xv[2] + xv[3]);
  float s = st[0] + st[1] + x8;
  s += dpp<XOR1>(s); s += dpp<XOR2>(s); s += dpp<ROR4>(s); s += dpp<ROR8>(s); s = xrow_sum(s);
  mean = s * (1.f / DM);
  f32x2 mm = {mean, mean};
  asm volatile("" : "+v"(mm));
#pragma unroll
  for (int i = 0; i < 4; ++i) xv[i] -= mm;
  const float e8 = x8 - mean;
  const f32x2 qt = (xv[0] * xv[0] + xv[1] * xv[1]) + (xv[2] * xv[2] + xv[3] * xv[3]);
  float q = fmaf(e8, e8, qt[0] + qt[1]);
  q += dpp<XOR1>(q); q += dpp<XOR2>(q); q += dpp<ROR4>(q); q += dpp<ROR8>(q); q = xrow_sum(q);
  rstd = __builtin_amdgcn_rsqf(q * (1.f / DM) + 1e-5f);
}
__device__ __forceinline__ unsigned next_tag(unsigned e) {
  unsigned h = e * 0x9E3779B1u + 0x7F4A7C15u;
  h = (h ^ (h >> 15)) | 0x01010101u;
  return ((e & 0x7f7f7f7fu) + (h & 0x7f7f7f7fu)) ^ ((e ^ h) & 0x80808080u);
}
__device__ __forceinline__ v4u pack4(f32x4 x, unsigned tag) {
  return v4u{__builtin_amdgcn_perm(asu(x[0]), tag, 0x07060500u), __builtin_amdgcn_perm(asu(x[1]), tag, 0x07060501u),
             __builtin_amdgcn_perm(asu(x[2]), tag, 0x07060502u), __builtin_amdgcn_perm(asu(x[3]), tag, 0x07060503u)};
}
__device__ __forceinline__ float q24(float x) { return asf(asu(x) & 0xffffff00u); }
__device__ __forceinline__ f32x4 unpack4(v4u v) { return f32x4{asf(v[0] & 0xffffff00u), asf(v[1] & 0xffffff00u), asf(v[2] & 0xffffff00u), asf(v[3] & 0xffffff00u)}; }
__device__ __forceinline__ bool tagged(v4u v, unsigned tag) {
  const unsigned lo = __builtin_amdgcn_perm(v[1], v[0], 0x0c0c0400u), hi = __builtin_amdgcn_perm(v[3], v[2], 0x04000c0cu);
  return (lo | hi) == tag;
}
template <int N, int STRIDE, int SLEEP> __device__ __forceinline__ f32x4 gather_sum(__amdgpu_buffer_rsrc_t rws, int off, unsigned tag) {
  v4u t[N];
  bool good;
  unsigned spins = 0;
#pragma unroll 1
  do {
    if (SLEEP && spins) __builtin_amdgcn_s_sleep(SLEEP);
    CBAR();
#pragma unroll
    for (int i = 0; i < N; ++i) t[i] = ld16<AUX_SC1>(rws, off, i * STRIDE);
    good = true;
#pragma unroll
    for (int i = 0; i < N; ++i) good &= tagged(t[i], tag);
  } while (!good && ++spins < SPIN_CAP);
  f32x4 s = unpack4(t[0]);
  if (N == 4) s = (s + unpack4(t[1])) + (unpack4(t[2]) + unpack4(t[3]));
  else {
#pragma unroll
    for (int i = 1; i < N; ++i) s += unpack4(t[i]);
  }
  if (!good) s[0] = __builtin_nanf("");
  return s;
}
__device__ __forceinline__ f32x4 project(v4u af, const v4u (&wf)[8], int lane) {
  f32x4 d[8];
#pragma unroll
  for (int k = 0; k < 4; ++k) { d[k] = mfma(af, wf[k], f32x4{0.f, 0.f, 0.f, 0.f}); SCHED(); d[4 + k] = mfma(af, wf[4 + k], f32x4{0.f, 0.f, 0.f, 0.f}); SCHED(); }
  const bool up = (lane & 8) != 0;
  f32x4 u;
#pragma unroll
  for (int k = 0; k < 4; ++k) {
    asm volatile("" : "+v"(d[k]), "+v"(d[4 + k]));
    const float keep = up ? d[4 + k][1] : d[k][0], send = up ? d[k][1] : d[4 + k][0];
    SCHED();
    u[k] = keep + dpp<ROR8>(send);
    SCHED();
  }
  return u;
}

__global__ void __launch_bounds__(NTHR, 3) fused_kernel(Args p) {
  const int blk = blockIdx.x, idx = blk >> 3;
  const unsigned long long tbase = ((const __attribute__((address_space(4))) unsigned long long*)__builtin_amdgcn_kernarg_segment_ptr())
      [(((idx & 16) ? 0x2121AA2198217621ull : 0x5021442133213321ull) >> (4 * (idx & 15))) & 15u];
  SCHED();
  const unsigned pf = ld4<0>(mkrs((const void*)(__builtin_amdgcn_s_getpc() & ~255ull), CODE_PF), ((int)threadIdx.x - 384) * 64);
  SCHED();
  __shared__ __attribute__((aligned(16))) unsigned char smem[L_BYTES];
  LAS unsigned char* L = (LAS unsigned char*)smem;
  volatile LAS unsigned* fl = (volatile LAS unsigned*)(L + L_FL);
  const int tid = threadIdx.x, lane = tid & 63, wave = __builtin_amdgcn_readfirstlane(tid >> 6);
  const int c = lane & 15, g = lane >> 4;
  const int kvh = 2 * (blk & 7) + (idx >> 4), sub = idx & 15, b = sub >> 2, split = sub & 3, pair = b * 16 + kvh;
  unsigned tch, tstart;
  unsigned long long t64;
  {
    const unsigned kind = (unsigned)(((idx & 16) ? 0x9911801100110011ull : 0x0011A21164114011ull) >> (4 * (idx & 15)));
    unsigned off = (kind & 1u) * ((unsigned)(pair * SK + (SK - WIN)) * 256u) + ((kind >> 3) & 1u) * 524286u;
    off += ((kind & 6u) == 2u ? (blk & 7) * 512u * (DM * 2u) : 0u) + ((kind & 6u) == 4u ? (2u << 20) << (idx >> 2) : 0u) + ((kind & 6u) == 6u ? DM * 8192u - 2u : 0u);
    int tvo = (lane == 0 && wave < 4) ? 0 : OOB;
    asm volatile("" : "+s"(off), "+v"(tvo));
    SCHED();
    const __amdgpu_buffer_rsrc_t rt = mkrs((const void*)(tbase + off), 2);
    asm volatile("" :: "s"(rt));
    t64 = __builtin_amdgcn_s_memrealtime();
    SCHED();
    tch = __builtin_amdgcn_raw_buffer_load_b16(rt, tvo, 0, AUX_SC1);
  }
  SCHED();
  const __amdgpu_buffer_rsrc_t rws = mkrs(p.ws, WS_END);
#define INIT_BARRIER() do { SCHED(); asm volatile("" : "+s"(t64)); tstart = (unsigned)t64; if (tid < 16) fl[tid] = tid == F_END ? tstart + T_FIRST + CUM(31) / 4 : (tid == F_ORG ? tstart : 0u); \
    LDS_DRAIN(); __builtin_amdgcn_s_barrier(); CBAR(); tstart = __builtin_amdgcn_readfirstlane(fl[F_ORG]); SCHED(); } while (0)
  const int ngrp = b == 3 ? 3 : 2;

  if (wave >= 4) {
    const int a = wave - 4;
    const size_t kvoff = ((size_t)pair * SK + (SK - WIN) + split * 512 + a * 64) * 256;
    const __amdgpu_buffer_rsrc_t rk = mkrs(p.k + kvoff, 16384), rv = mkrs(p.v + kvoff, 16384);
    const unsigned ldsk = (unsigned)(uintptr_t)(L + L_K) + a * 16384;
    const int hr = ((idx + 2 * a + 2 * (blk & 7)) & 15) << 10;
    int kv[4];
#pragma unroll
    for (int r = 0; r < 4; ++r) kv[r] = g * 256 + ((c ^ (4 * g + r)) << 4);
    __builtin_amdgcn_s_setprio(2);
    v4u wr[8];
    const __amdgpu_buffer_rsrc_t ro = mkrs(p.wo, a < ngrp ? 4096 * DM * 2 : 0);
    const int wo_off = (kvh * 256 + 64 * split + 8 * (4 * (c >> 3) + g)) * (DM * 2) + (2 * b + a) * 128 + (c & 7) * 16;
#pragma unroll
    for (int jj = 0; jj < WO_EARLY; ++jj) wr[jj] = ld16<AUX_NT>(ro, wo_off, jj * (DM * 2));
    INIT_BARRIER();
    unsigned tref = tstart + T_FIRST;
    {
      const int slow = (int)(NOW() + B_GAP - tref);
      tref += slow < 0 ? 0 : (slow > B_MAX ? B_MAX : slow);
    }
    int scale = 256;
    const int stag = (a - 7) * T_STAG;
#define RESPREAD(n) do { const unsigned at_ = tref + (CUM(n) * scale >> 10); \
      int s_ = ((int)(__builtin_amdgcn_readfirstlane(fl[F_END]) - at_) * ((1 << 22) / (CUM(31) - CUM(n)))) >> 12; \
      scale = s_ < SC_MIN ? SC_MIN : (s_ > SC_MAX ? SC_MAX : s_); tref = at_ - (CUM(n) * scale >> 10); } while (0)
#define SLOT(n) do { SCHED(); if ((n) == 4 || (n) == 8 || (n) == 12 || (n) == 16 || (n) == 27) RESPREAD((n) - 1); \
      while ((int)(NOW() - tref - stag) < (CUM(n) * scale >> 10)) { if ((n) < 31) __builtin_amdgcn_s_sleep(1); } SCHED(); } while (0)
#define V_AT(i, n) do { SLOT(n); vr[i] = ld16<AUX_NT>(rv, lane * 16, ((i) << 10) ^ hr); VM_WAIT(WINDOW); } while (0)
#define V_PIECE(i) V_AT(i, 16 + (i))
#pragma unroll
    for (int i = 0; i < 16; ++i) {
      SLOT(i);
      unsigned keep;
      asm volatile("s_mov_b32 %0, m0\n\ts_mov_b32 m0, %1\n\ts_nop 0\n\tbuffer_load_dwordx4 %2, %3, %4 offen nt lds\n\ts_mov_b32 m0, %0"
                   : "=&s"(keep) : "s"(ldsk + i * 1024), "v"(kv[i & 3]), "s"(rk), "s"((i << 10) ^ hr) : "memory");
      VM_WAIT(WINDOW);
    }
    v4u vr[16];
#pragma unroll
    for (int i = 0; i < QKV; ++i) V_PIECE(i);
    V_AT(QKV, 15 + QKV);
    asm volatile("" :: "v"(pf));
    asm volatile("" :: "v"(tch));
    lds_wait_ge<1>(fl + F_Q, 1u);
    v4u qf[4];
#pragma unroll
    for (int kk = 0; kk < 4; ++kk) qf[kk] = *(const LAS v4u*)(L + L_Q + ((c >> 2) & 1) * 256 + kk * 64 + g * 16);
    VM_WAIT(QKV + 1);
    f32x4 S[4];
    {
      const LAS unsigned char* kb = L + L_K + a * 16384 + (c & 3) * 1024 + (c >> 2) * 256;
#pragma unroll
      for (int t = 0; t < 4; ++t) S[t] = f32x4{0.f, 0.f, 0.f, 0.f};
#pragma unroll
      for (int kk = 0; kk < 4; ++kk) {
        v4u kf[4];
#pragma unroll
        for (int t = 0; t < 4; ++t) kf[t] = *(const LAS v4u*)(kb + t * 4096 + (((4 * kk) ^ g ^ c) << 4));
#pragma unroll
        for (int t = 0; t < 4; ++t) S[t] = mfma(kf[t], qf[kk], S[t]);
      }
    }
    asm volatile("" : "+v"(S[0]), "+v"(S[1]), "+v"(S[2]), "+v"(S[3]));
    V_PIECE(QKV + 1);
    constexpr float SC = 0.088388347648318440f * 1.4426950408889634f;
    float mx = -INFINITY;
#pragma unroll
    for (int t = 0; t < 4; ++t) mx = fmaxf(mx, fmaxf(fmaxf(S[t][0], S[t][1]), fmaxf(S[t][2], S[t][3])));
    mx = xrow_max(mx) * SC;
    float ls = 0.f;
    v4u pr[2];
#pragma unroll
    for (int t = 0; t < 4; ++t) {
      float e[4];
#pragma unroll
      for (int r = 0; r < 4; ++r) e[r] = __builtin_amdgcn_exp2f(fmaf(S[t][r], SC, -mx));
      ls += (e[0] + e[1]) + (e[2] + e[3]);
      pr[t >> 1][2 * (t & 1)] = cvtpk(e[0], e[1]);
      pr[t >> 1][2 * (t & 1) + 1] = cvtpk(e[2], e[3]);
    }
    ls = xrow_sum(ls);
    if ((lane & 0x3b) == 0) *(LAS f32x2*)(L + L_ML + (lane >> 2) * 64 + a * 8) = f32x2{mx, ls};
    lds_arrive(L, F_ML, lane);
    asm volatile("" : "+v"(pr[0]), "+v"(pr[1]));
    V_PIECE(QKV + 2);
    unsigned oaddr = L_O + a * 1024 + (g & 1) * 512 + c * 32 + (g >> 1) * 16;
    asm volatile("" : "+v"(oaddr));
#pragma unroll
    for (int i = QKV + 3; i < 13; ++i) V_PIECE(i);
    f32x4 acc[8];
#pragma unroll
    for (int j = 0; j < 8; ++j) { acc[j] = mfma(pr[0], colfrag(vr, j), f32x4{0.f, 0.f, 0.f, 0.f}); asm volatile("" : "+v"(acc[j])); }
    v4u vf[8];
#define V_PAIR(d) do { _Pragma("unroll") for (int j = 0; j < 8; ++j) { \
      unsigned t_ = __builtin_amdgcn_perm(vr[9 + 2 * (d)][j >> 1], vr[8 + 2 * (d)][j >> 1], (j & 1) ? 0x07060302u : 0x05040100u); \
      asm volatile("" : "+v"(t_)); vf[j][d] = t_; } SCHED(); } while (0)
    V_PIECE(13);
    V_PIECE(14);
    V_PAIR(0);
    V_PIECE(15);
    if (lane == 0) __hip_atomic_fetch_add((LAS unsigned*)(L + L_FL + 4 * F_VI), 1u, RLX_WG);
    SCHED();
    V_PAIR(1);
    V_PAIR(2);
    auto finish = [&]() {
      V_PAIR(3);
#pragma unroll
      for (int j = 0; j < 8; ++j) acc[j] = mfma(pr[1], vf[j], acc[j]);
      asm volatile("" : "+v"(acc[0]), "+v"(acc[1]), "+v"(acc[2]), "+v"(acc[3]), "+v"(acc[4]), "+v"(acc[5]), "+v"(acc[6]), "+v"(acc[7]));
      const bool hi = (lane & 32) != 0;
      *(LAS f32x4*)(L + oaddr) = f32x4{hi ? acc[4][0] : acc[0][0], hi ? acc[5][0] : acc[1][0], hi ? acc[6][0] : acc[2][0], hi ? acc[7][0] : acc[3][0]};
      CBAR();
      if (lane == 0) __hip_atomic_fetch_add((LAS unsigned*)(L + L_FL + 4 * F_O), 1u, RLX_WG);
      SCHED();
    };
    if (a >= ngrp) {
      finish();
      if (a == 3) {
        const unsigned wm = ld4<AUX_SC1>(rws, lane < 8 * ngrp ? WS_SP + (kvh * 4 + split) * 9216 + 2 * b * 1024 + lane * 128 : OOB);
        asm volatile("" :: "v"(wm));
      }
      return;
    }
    {
      lds_wait_ge<1>(fl + F_VI, 8u);
      __builtin_amdgcn_s_sleep(WO_AT);
#pragma unroll
      for (int jj = WO_EARLY; jj < 8; ++jj) wr[jj] = ld16<AUX_NT>(ro, wo_off, jj * (DM * 2));
      SCHED();
      finish();
      const unsigned tag = __builtin_amdgcn_readfirstlane(fl[F_TAG]);
      const bool ph = (c & 2) != 0;
      int oo = WS_OP + ((c >> 2) * 16 + kvh) * 4 * OP_SRC + (64 * split + 8 * (4 * (c & 1) + g) + (c & 2) * 2) * 4;
      int so = WS_SP + (kvh * 4 + split) * 9216 + (2 * b + a) * 1024 + g * 256 + (c & 7) * 32 + (c >> 3) * 16;
      asm volatile("" : "+v"(so), "+v"(oo));
      lds_wait_ge<0>(fl + F_O, 8u);
      __builtin_amdgcn_s_sleep(O_DELAY);
      CBAR();
      v4u t[4];
#pragma unroll
      for (int i = 0; i < 4; ++i) t[i] = ld16<AUX_SC1>(rws, oo, i * OP_SRC);
      SCHED();
      v4u wf[8];
#pragma unroll
      for (int j = 0; j < 8; ++j) { wf[j] = colfrag(wr, j); asm volatile("" : "+v"(wf[j])); }
      SCHED();
      f32x4 x = (unpack4(t[0]) + unpack4(t[1])) + (unpack4(t[2]) + unpack4(t[3]));
      if (!(tagged(t[0], tag) & tagged(t[1], tag) & tagged(t[2], tag) & tagged(t[3], tag))) x = gather_sum<4, OP_SRC, 0>(rws, oo, tag);
      const unsigned p0 = cvtpk(x[0], x[1]), p1 = cvtpk(x[2], x[3]);
      const unsigned q0 = dppu<XOR2>(p0), q1 = dppu<XOR2>(p1);
      const v4u af = {ph ? q0 : p0, ph ? q1 : p1, ph ? p0 : q0, ph ? p1 : q1};
      st16_sc1(rws, so, pack4(project(af, wf, lane), tag));
      if (lane == 0) __hip_atomic_fetch_add((LAS unsigned*)(L + L_FL + 4 * F_GATH), 1u, RLX_WG);
    }
    return;
  }

  __builtin_amdgcn_s_setprio(3);
  const bool qrole = (sub < 12) && (wave < 3);
  const unsigned epoch = ld4<AUX_SC1>(rws, (wave == 3 && lane == 0) ? WS_EPOCH : OOB);
  const __amdgpu_buffer_rsrc_t rx = mkrs(p.x + wave * (DM * 2), DM * 2);
  v4u xr = ld16<0>(rx, lane * 16);
  unsigned xt = ld2(rx, 1024 + lane * 2);
  const int mt = tid & 127;
  v4u m1, mb, m2;
  unsigned m1t, mbt, m2t, tag, warm, b2v = 0u;
  auto more_loads = [&]() {
    SCHED();
    const int fo = (blk & 3) * 2304;
    const __amdgpu_buffer_rsrc_t r1 = mkrs(p.w1 + fo, 2304), rb = mkrs(p.b1 + fo, 2304), r2 = mkrs(p.w2 + fo, 2304);
    m1t = ld2(r1, 2048 + mt * 2); mbt = ld2(rb, 2048 + mt * 2); m2t = ld2(r2, 2048 + mt * 2);
    m1 = ld16<0>(r1, mt * 16); mb = ld16<0>(rb, mt * 16); m2 = ld16<0>(r2, mt * 16);
    b2v = ld2(mkrs(p.b2, 2), wave == 3 ? 0 : OOB);
    warm = ld4<AUX_SC1>(rws, (wave == 3 && lane < 11) ? (lane < 9 ? WS_OP + (pair * 4 + split) * OP_SRC : WS_YP + blk * 256 - 9 * 128) + lane * 128 : OOB);
    SCHED();
  };
  auto layernorm = [&]() {
    INIT_BARRIER();
    asm volatile("" : "+v"(xr), "+v"(xt));
    tag = next_tag(__builtin_amdgcn_readfirstlane(epoch));
    if (wave == 3 && idx == 0) {
      const int slow = (int)(NOW() - tstart) - A0;
      st16_sc1(rws, lane < 32 ? WS_TB + blk * 4096 + lane * 128 : OOB, pack4(f32x4{asf((tstart + (slow > 0 ? slow * AW >> 3 : 0)) << 8), 0.f, 0.f, 0.f}, tag));
    }
    float mean, rstd;
    row_stats(xr, xt, mean, rstd);
    f32x2 mm = {mean, mean}, rr = {rstd, rstd};
    asm volatile("" : "+v"(mm), "+v"(rr));
    v4u nr;
#pragma unroll
    for (int i = 0; i < 4; ++i) { const f32x2 n = (f32x2{bflo(xr[i]), bfhi(xr[i])} - mm) * rr; nr[i] = cvtpk(n[0], n[1]); }
    const unsigned nt = cvtpk((bflo(xt) - mean) * rstd, 0.f);
    LAS unsigned char* row = L + wave * (DM * 2);
    *(LAS v4u*)(row + L_XN + lane * 16) = nr;
    *(LAS unsigned short*)(row + L_XN + 1024 + lane * 2) = (unsigned short)nt;
    *(LAS v4u*)(row + L_X + lane * 16) = xr;
    *(LAS unsigned short*)(row + L_X + 1024 + lane * 2) = (unsigned short)xt;
    if (wave == 3 && lane == 0) fl[F_TAG] = tag;
    lds_arrive(L, F_LN, lane);
    asm volatile("" :: "v"(pf));
    asm volatile("" :: "v"(tch));
    lds_wait_ge<0>(fl + F_LN, 4u);
    tag = __builtin_amdgcn_readfirstlane(fl[F_TAG]);
  };
  if (__builtin_expect(qrole, 1)) {
    const int qi = (idx >> 4) * 12 + sub, ks = qi >> 3, G = 8 * (qi & 7) + ((blk + qi) & 7);
    const int R0 = 192 * ks + 64 * wave;
    v4u wr[8];
    const __amdgpu_buffer_rsrc_t rq = mkrs(p.wq, DM * 4096 * 2);
    const int off = (R0 + 8 * (4 * (c >> 3) + g)) * 8192 + G * 128 + (c & 7) * 16;
#pragma unroll
    for (int jj = 0; jj < 8; ++jj) wr[jj] = ld16<AUX_NT>(rq, off, jj * 8192);
    more_loads();
    layernorm();
    const v4u af = *(const LAS v4u*)(L + L_XN + (c >> 2) * (DM * 2) + (R0 + 8 * (4 * (c & 1) + g)) * 2);
    v4u wf[8];
#pragma unroll
    for (int j = 0; j < 8; ++j) wf[j] = colfrag(wr, j);
    *(LAS f32x4*)(L + L_QP + wave * 1024 + g * 256 + (c & 7) * 32 + (c >> 3) * 16) = project(af, wf, lane);
    lds_arrive(L, F_QP, lane);
    if (wave == 2) {
      lds_wait_ge<0>(fl + F_QP, 3u);
      const LAS f32x4* sq = (const LAS f32x4*)(L + L_QP + lane * 16);
      st16_sc1(rws, WS_QP + (((G >> 2) * 4 + g) * 3 + ks) * 1024 + (G & 3) * 256 + c * 16, pack4(sq[0] + sq[64] + sq[128], tag));
    }
  } else {
    more_loads();
    layernorm();
    asm volatile("" :: "v"(m1), "v"(mb), "v"(m2), "v"(m1t), "v"(mbt), "v"(m2t), "v"(b2v), "v"(warm));
  }
  __builtin_amdgcn_s_setprio(0);
  {
    const int ch0 = 36 * (blk >> 2) + 18 * (wave >> 1);
    float mean, rstd;
    row_stats(*(const LAS v4u*)(L + L_XN + (blk >> 6) * (DM * 2) + lane * 16), *(const LAS unsigned short*)(L + L_XN + (blk >> 6) * (DM * 2) + 1024 + lane * 2), mean, rstd);
    const unsigned xh = *(const LAS unsigned*)(L + L_XN + (ch0 + 2 * (c < 9 ? c : 0)) * 2);
    const float h0 = (bflo(xh) - mean) * rstd, h1 = (bfhi(xh) - mean) * rstd;
    float w1[9], bb[9], w2[9];
#pragma unroll
    for (int i = 0; i < 4; ++i) {
      w1[2 * i] = bflo(m1[i]); w1[2 * i + 1] = bfhi(m1[i]); bb[2 * i] = bflo(mb[i]); bb[2 * i + 1] = bfhi(mb[i]); w2[2 * i] = bflo(m2[i]); w2[2 * i + 1] = bfhi(m2[i]);
    }
    w1[8] = bflo(m1t); bb[8] = bflo(mbt); w2[8] = bflo(m2t);
    constexpr float C1 = -2.0f * 0.7978845608028654f * 1.4426950408889634f, C2 = C1 * 0.044715f;
    float ykeep = 0.f;
    bool have_q = wave != 1;
    int look = 2;
    asm volatile("" :: "v"(warm));
#pragma unroll 1
    for (int it = 0; it < 9; ++it) {
      if ((wave == 3 && it == 0) || (wave == 2 && it == look)) {
        CBAR();
        const int guess = (int)(NOW() - tstart) - T_VIS;
        const v4u tb = ld16<AUX_SC1>(rws, lane < 8 ? WS_TB + lane * 4096 + idx * 128 : OOB);
        const bool seen = tagged(tb, tag);
        if ((__ballot(seen) & 255u) != 255u) look += 2;
        float d = seen ? (float)((int)((tb[0] & 0xffffff00u) - (tstart << 8)) >> 8) : (float)(guess > 0 ? guess : 0);
        float dm = seen ? d : -1e9f;
        dm = fmaxf(dm, dpp<XOR1>(dm)); dm = fmaxf(dm, dpp<XOR2>(dm)); dm = fmaxf(dm, dpp<XOR7>(dm));
        d += dpp<XOR1>(d); d += dpp<XOR2>(d); d += dpp<XOR7>(d);
        if (lane == 0) fl[F_END] = tstart + (int)fmaxf(d * 0.125f, dm - (float)T_CATCH) + T_MARGIN + T_FIRST + CUM(31) / 4;
      }
      if (!have_q && (it == 8 || (int)(NOW() + (T_FIRST + CUM(31) / 4 - Q_AT) - __builtin_amdgcn_readfirstlane(fl[F_END])) >= 0)) {
        have_q = true;
        __builtin_amdgcn_s_setprio(3);
        const f32x4 qs = gather_sum<3, 1024, 8>(rws, WS_QP + (kvh * 4 + b) * 3 * 1024 + lane * 16, tag);
        *(LAS v2u*)(L + L_Q + lane * 8) = v2u{cvtpk(qs[0], qs[1]), cvtpk(qs[2], qs[3])};
        lds_arrive(L, F_Q, lane);
        __builtin_amdgcn_s_setprio(0);
      }
      const f32x2 h = {rdlane(h0, it), rdlane(h1, it)};
      f32x2 acc = {0.f, 0.f};
#pragma unroll
      for (int i = 0; i < 9; ++i) {
        const f32x2 u = h * w1[i] + bb[i];
        const f32x2 z = u * (u * u * C2 + C1);
        const f32x2 d = f32x2{__builtin_amdgcn_exp2f(z[0]), __builtin_amdgcn_exp2f(z[1])} + 1.0f;
        const f32x2 r = {__builtin_amdgcn_rcpf(d[0]), __builtin_amdgcn_rcpf(d[1])};
        acc = (u * w2[i]) * r + acc;
      }
      const auto sw = __builtin_amdgcn_permlane32_swap(asu(acc[0]), asu(acc[1]), false, false);
      float y = asf(sw[0]) + asf(sw[1]);
      y += dpp<XOR1>(y); y += dpp<XOR2>(y); y += dpp<ROR4>(y); y += dpp<ROR8>(y);
      ykeep = (c == it) ? y : ykeep;
    }
    __builtin_amdgcn_s_setprio(3);
    const auto sy = __builtin_amdgcn_permlane16_swap(asu(ykeep), asu(ykeep), false, false);
    ykeep = asf(sy[0]) + asf(sy[1]);
    if (c < 9 && (g & 1) == 0) *(LAS float*)(L + L_MIX + (wave & 1) * 256 + (18 * (wave >> 1) + 2 * c + (g >> 1)) * 4) = ykeep;
    lds_arrive(L, F_MIX, lane);
    if (wave == 3) {
      lds_wait_ge<2>(fl + F_MIX, 4u);
      const LAS f32x4* sy4 = (const LAS f32x4*)(L + L_MIX + c * 16);
      st16_sc1(rws, lane < 16 ? WS_YP + blk * 256 + lane * 16 : OOB, pack4(sy4[0] + sy4[16], tag));
    }
  }

  if (wave == 0) {
    lds_wait_ge<1>(fl + F_ML, 8u);
    const f32x2 ml = *(const LAS f32x2*)(L + L_ML + (lane >> 5) * 64 + (lane & 7) * 8);
    float M = fmaxf(ml[0], dpp<XOR1>(ml[0])); M = fmaxf(M, dpp<XOR2>(M)); M = fmaxf(M, dpp<XOR7>(M));
    M = q24(M);
    float Ls = __builtin_amdgcn_exp2f(ml[0] - M) * ml[1];
    Ls += dpp<XOR1>(Ls); Ls += dpp<XOR2>(Ls); Ls += dpp<XOR7>(Ls);
    const int ob = WS_OP + pair * 4 * OP_SRC;
    st16_sc1(rws, lane == 0 ? ob + split * OP_SRC + 1024 : OOB, pack4(f32x4{M, Ls, rdlane(M, 32), rdlane(Ls, 32)}, tag));
    __builtin_amdgcn_s_sleep(ML_SLEEP);
    const f32x4 st4 = gather_sum<1, 0, ML_RETRY>(rws, ob + (lane & 3) * OP_SRC + 1024, tag);
    const float ms = lane < 32 ? st4[0] : st4[2];
    float Mg = fmaxf(ms, dpp<XOR1>(ms)); Mg = fmaxf(Mg, dpp<XOR2>(Mg));
    float Lg = __builtin_amdgcn_exp2f(ms - Mg) * (lane < 32 ? st4[1] : st4[3]);
    Lg += dpp<XOR1>(Lg); Lg += dpp<XOR2>(Lg);
    const float w = __builtin_amdgcn_exp2f(ml[0] - Mg) * __builtin_amdgcn_rcpf(Lg);
    float ww[8] = {dpp<BCAST>(w), dpp<BCAST + 1>(w), dpp<BCAST + 2>(w), dpp<BCAST + 3>(w), dpp<BCAST + 4>(w), dpp<BCAST + 5>(w), dpp<BCAST + 6>(w), dpp<BCAST + 7>(w)};
    int po = ob + split * OP_SRC + lane * 16;
    asm volatile("" : "+v"(po), "+v"(ww[0]), "+v"(ww[1]), "+v"(ww[2]), "+v"(ww[3]), "+v"(ww[4]), "+v"(ww[5]), "+v"(ww[6]), "+v"(ww[7]));
    lds_wait_ge<2>(fl + F_O, 1u);
    lds_wait_ge<0>(fl + F_O, 8u);
    const LAS f32x4* o = (const LAS f32x4*)(L + L_O + lane * 16);
    const f32x4 t = ((o[0] * ww[0] + o[64] * ww[1]) + (o[128] * ww[2] + o[192] * ww[3])) + ((o[256] * ww[4] + o[320] * ww[5]) + (o[384] * ww[6] + o[448] * ww[7]));
    st16_sc1(rws, po, pack4(t, tag));
  } else if (wave == 3 && idx >= 12 && (idx >> 1) != 13) {
    const int ci = (idx - (idx >= 28 ? 14 : 12)) * 8 + (blk & 7), bc = ci / 36, cg = ci % 36;
    const int chn = 4 * (lane & 3) + 2 * (g & 1) + (g >> 1);
    for (unsigned spins = 0; (int)(NOW() + Y_LEAD - __builtin_amdgcn_readfirstlane(fl[F_END])) < 0 && spins < 4096u; ++spins) __builtin_amdgcn_s_sleep(4);
    const int yu = 4 * ci + (lane & 3);
    const f32x4 y4 = gather_sum<1, 0, 16>(rws, WS_YP + (4 * (yu / 9) + (c >> 2)) * 256 + (yu % 9) * 16, tag);
    float yy = (g & 1) ? ((g >> 1) ? y4[3] : y4[2]) : ((g >> 1) ? y4[1] : y4[0]);
    yy += dpp<ROR4>(yy); yy += dpp<ROR8>(yy);
    yy += bflo(b2v);
    float gt = yy * __builtin_amdgcn_rcpf(1.f + __builtin_amdgcn_exp2f(-1.4426950408889634f * yy));
    float xo = bflo(*(const LAS unsigned short*)(L + L_X + (16 * ci + chn) * 2));
    int so = WS_SP + (lane >> 2) * 4 * 9216 + (cg >> 2) * 1024 + bc * 256 + (cg & 3) * 64 + (lane & 3) * 16;
    int oo = lane < 4 ? (16 * ci + 4 * lane) * 2 : OOB;
    asm volatile("" : "+v"(gt), "+v"(xo), "+v"(so), "+v"(oo));
    lds_wait_ge<1>(fl + F_GATH, (unsigned)ngrp);
    if (S_DELAY) __builtin_amdgcn_s_sleep(S_DELAY);
    const f32x4 t = gather_sum<4, 9216, 0>(rws, so, tag);
    const auto p01 = __builtin_amdgcn_permlane32_swap(asu(t[0]), asu(t[1]), false, false), p23 = __builtin_amdgcn_permlane32_swap(asu(t[2]), asu(t[3]), false, false);
    const auto pq = __builtin_amdgcn_permlane16_swap(asu(asf(p01[0]) + asf(p01[1])), asu(asf(p23[0]) + asf(p23[1])), false, false);
    float r = asf(pq[0]) + asf(pq[1]);
    r += dpp<ROR4>(r); r += dpp<ROR8>(r);
    const unsigned ov = asu(fmaf(r, gt, xo));
    const auto o2 = __builtin_amdgcn_permlane32_swap(ov, ov, false, false);
    const unsigned pk = cvtpk(asf(o2[0]), asf(o2[1]));
    const auto o4 = __builtin_amdgcn_permlane16_swap(pk, pk, false, false);
    __builtin_amdgcn_raw_buffer_store_b64(v2u{o4[0], o4[1]}, mkrs(p.out, 4 * DM * 2), oo, 0, AUX_SC1);
    if (ci == 0 && lane == 0) __hip_atomic_store((GAS unsigned*)(p.ws + WS_EPOCH), tag, RLX_AGENT);
  }
}
}

extern "C" void kernel_launch(void* const* d_in, const int* in_sizes, int n_in, void* d_out, int out_size, void* d_ws, size_t ws_size,
                              hipStream_t stream) {
  Args a;
  a.x = (const unsigned char*)d_in[0]; a.k = (const unsigned char*)d_in[1]; a.v = (const unsigned char*)d_in[2];
  a.wq = (const unsigned char*)d_in[3]; a.wo = (const unsigned char*)d_in[4];
  a.w1 = (const unsigned char*)d_in[6]; a.b1 = (const unsigned char*)d_in[7]; a.w2 = (const unsigned char*)d_in[8]; a.b2 = (const unsigned char*)d_in[9];
  a.out = (unsigned char*)d_out; a.ws = (unsigned char*)d_ws;
  hipLaunchKernelGGL(fused_kernel, dim3(256), dim3(NTHR), 0, stream, a);
}
```
